```python
import torch

def _softmax(x, axis=-1):
    return torch.softmax(x, dim=axis)

def _gelu(x):
    return torch.nn.functional.gelu(x, approximate='tanh')

def _silu(x):
    return torch.nn.functional.silu(x)

def _sigmoid(x):
    return torch.sigmoid(x)

def _layernorm(x, axis=-1, eps=1e-05):
    m = x.mean(dim=axis, keepdim=True)
    v = x.var(dim=axis, keepdim=True, unbiased=False)
    return (x - m) / torch.sqrt(v + eps)

def _scatter_add(tgt, idx, src, axis=0):
    return tgt.clone().index_add_(axis, idx, src)

def _segment_sum(src, seg, n):
    out = torch.zeros((n,) + src.shape[1:], dtype=src.dtype)
    return out.index_add_(0, seg, src)

def _pad(x, pw):
    flat = tuple((v for pair in reversed(pw) for v in pair))
    return torch.nn.functional.pad(x, flat)
kernel_name = 'adaLN_cond__compressed_kv_cla__geglu_ffn__decode'

def setup_inputs():

    _w = lambda a: (lambda t: t.to(torch.bfloat16) if t.is_floating_point() else t)(torch.as_tensor(a))

    def _A_setup():
        B, H, Sq, Sk, D, M = (1, 32, 1, 16384, 128, 576)
        Dc = 4 * D
        return {'x': _w((lambda *s: torch.randn(*s, dtype=torch.bfloat16) * 0.05)(B, Sq, M)), 'c_kv': _w((lambda *s: torch.randn(*s, dtype=torch.bfloat16) * 1.6)(B, Sk, Dc)), 'w_q': _w((lambda *s: torch.randn(*s, dtype=torch.bfloat16) * 0.8)(M, H, D) * M ** (-0.5)), 'w_uk': _w((lambda *s: torch.randn(*s, dtype=torch.bfloat16) * 0.8)(Dc, H, D) * Dc ** (-0.5)), 'w_uv': _w((lambda *s: torch.randn(*s, dtype=torch.bfloat16) * 1.6)(Dc, H, D) * Dc ** (-0.5)), 'w_o': _w((lambda *s: torch.randn(*s, dtype=torch.bfloat16) * 1.6)(H, D, M) * (H * D) ** (-0.5))}
    _A_in = _A_setup()

    def _B_setup():
        B, S, D, F = (1, 1, 576, 4608)
        return {'x': _w((lambda *s: torch.randn(*s, dtype=torch.bfloat16) * 0.05)(B, S, D)), 'w_gate': _w((lambda *s: torch.randn(*s, dtype=torch.bfloat16) * 0.05)(D, F)), 'w_up': _w((lambda *s: torch.randn(*s, dtype=torch.bfloat16) * 0.05)(D, F)), 'w_down': _w((lambda *s: torch.randn(*s, dtype=torch.bfloat16) * 0.05)(F, D))}
    _B_in = _B_setup()
    return {'A_x': _A_in['x'], 'A_c_kv': _A_in['c_kv'], 'A_w_q': _A_in['w_q'], 'A_w_uk': _A_in['w_uk'], 'A_w_uv': _A_in['w_uv'], 'A_w_o': _A_in['w_o'], 'B_x': _B_in['x'], 'B_w_gate': _B_in['w_gate'], 'B_w_up': _B_in['w_up'], 'B_w_down': _B_in['w_down'], 'c': _w((lambda *s: torch.randn(*s, dtype=torch.bfloat16) * 0.1)(1, 576) * 8.0), 'w_mod': _w((lambda *s: torch.randn(*s, dtype=torch.bfloat16) * 0.1)(576, 6 * 576) * 576 ** (-0.5) * 8.0)}

def reference(A_x, A_c_kv, A_w_q, A_w_uk, A_w_uv, A_w_o, B_x, B_w_gate, B_w_up, B_w_down, c, w_mod):

    def _A_ref(x, c_kv, w_q, w_uk, w_uv, w_o):
        B, Sq, M = (x.shape[0], x.shape[1], x.shape[2])
        Sk = c_kv.shape[1]
        H, D = (w_q.shape[1], w_q.shape[2])
        q = torch.einsum('bqm,mhd->bhqd', x, w_q)
        k = torch.einsum('bkc,chd->bhkd', c_kv, w_uk)
        v = torch.einsum('bkc,chd->bhkd', c_kv, w_uv)
        scores = torch.einsum('bhqd,bhkd->bhqk', q, k) * D ** (-0.5)
        off = Sk - Sq
        qi = torch.arange(Sq)[:, None]
        kj = torch.arange(Sk)[None, :]
        cmask = kj <= qi + off
        scores = torch.where(cmask[None, None], scores, -1000000000.0)
        p = _softmax(scores, axis=-1)
        ctx = torch.einsum('bhqk,bhkd->bhqd', p, v)
        return torch.einsum('bhqd,hdm->bqm', ctx, w_o)

    def _B_ref(x, w_gate, w_up, w_down):
        return _gelu(x @ w_gate) * (x @ w_up) @ w_down
    x = A_x
    mod = (_silu(c) @ w_mod).reshape(c.shape[0], 6, 1, -1)
    sa, ca, ga, sb, cb, gb = (mod[:, 0].clone(), mod[:, 1].clone(), mod[:, 2].clone(), mod[:, 3].clone(), mod[:, 4].clone(), mod[:, 5].clone())
    h = _layernorm(x, axis=-1) * (1 + ca) + sa
    x = x + ga * _A_ref(h, A_c_kv, A_w_q, A_w_uk, A_w_uv, A_w_o)
    h = _layernorm(x, axis=-1) * (1 + cb) + sb
    x = x + gb * _B_ref(h, B_w_gate, B_w_up, B_w_down)
    return x
```

```cpp
#include <hip/hip_runtime.h>
#include <hip/hip_bf16.h>
#define EMU 1
typedef unsigned short u16;
__device__ __forceinline__ float bf2f(u16 b) { return __builtin_bit_cast(float, (unsigned)b << 16); }
__device__ __forceinline__ u16 f2bf(float f) { unsigned u = __builtin_bit_cast(unsigned, f); u += 0x7FFFu + ((u >> 16) & 1u); return (u16)(u >> 16); }
__device__ __forceinline__ float rb(float f) { return EMU ? bf2f(f2bf(f)) : f; }

constexpr int M = 576, H = 32, D = 128, DC = 512, SK = 16384, F = 4608;

__device__ float block_sum(float v, float* red) {
    for (int o = 32; o > 0; o >>= 1) v += __shfl_xor(v, o);
    int w = threadIdx.x >> 6, nw = blockDim.x >> 6;
    __syncthreads();
    if ((threadIdx.x & 63) == 0) red[w] = v;
    __syncthreads();
    float s = 0; for (int i = 0; i < nw; ++i) s += red[i];
    return s;
}

__global__ void k_mod(const u16* c, const u16* w_mod, float* mod) {
    __shared__ float s[M];
    for (int i = threadIdx.x; i < M; i += blockDim.x) { float v = bf2f(c[i]); s[i] = rb(v / (1.f + __expf(-v))); }
    __syncthreads();
    int n = blockIdx.x * blockDim.x + threadIdx.x;
    if (n < 6 * M) { float a = 0; for (int k = 0; k < M; ++k) a += s[k] * bf2f(w_mod[(size_t)k * 6 * M + n]); mod[n] = rb(a); }
}
__device__ void ln_mod(const float* xs, const float* ss, const float* cc, float* h, float* red) {
    float a = 0; for (int i = threadIdx.x; i < M; i += blockDim.x) a += xs[i];
    float mean = block_sum(a, red) / M;
    float b = 0; for (int i = threadIdx.x; i < M; i += blockDim.x) { float d = xs[i] - mean; b += d * d; }
    float var = rb(block_sum(b, red) / M);
    mean = rb(mean);
    float den = rb(sqrtf(rb(var + 1e-5f)));
    for (int i = threadIdx.x; i < M; i += blockDim.x) {
        float n = rb(rb(xs[i] - mean) / den);
        h[i] = rb(rb(n * rb(1.f + cc[i])) + ss[i]);
    }
    __syncthreads();
}
__global__ void k_q(const u16* x, const float* mod, const u16* w_q, float* q) {
    __shared__ float xs[M], h[M], red[16];
    for (int i = threadIdx.x; i < M; i += blockDim.x) xs[i] = bf2f(x[i]);
    __syncthreads();
    ln_mod(xs, mod, mod + M, h, red);
    int n = blockIdx.x * blockDim.x + threadIdx.x;
    float a = 0; for (int k = 0; k < M; ++k) a += h[k] * bf2f(w_q[(size_t)k * H * D + n]);
    q[n] = rb(a);
}
__global__ void k_qabs(const float* q, const u16* w_uk, float* qa) {
    int gw = (blockIdx.x * blockDim.x + threadIdx.x) >> 6, lane = threadIdx.x & 63;
    int c = gw / H, h = gw % H;
    const u16* w = w_uk + ((size_t)c * H + h) * D;
    float a = q[h * D + lane] * bf2f(w[lane]) + q[h * D + 64 + lane] * bf2f(w[64 + lane]);
    for (int o = 32; o > 0; o >>= 1) a += __shfl_xor(a, o);
    if (lane == 0) qa[h * DC + c] = a * 0.08838834764831845f;
}
__global__ void k_attn(const float* qa, const u16* ckv, float* part, float* pm, float* pl) {
    __shared__ u16 kv[64][DC + 8];
    __shared__ float sc[H][64];
    int k0 = blockIdx.x * 64;
    for (int i = threadIdx.x; i < 64 * DC; i += 256) kv[i / DC][i % DC] = ckv[(size_t)k0 * DC + i];
    __syncthreads();
    for (int p = threadIdx.x; p < H * 64; p += 256) {
        int h = p / 64, k = p % 64; float a = 0;
        for (int c = 0; c < DC; ++c) a += qa[h * DC + c] * bf2f(kv[k][c]);
        sc[h][k] = a;
    }
    __syncthreads();
    if (threadIdx.x < H) {
        int h = threadIdx.x; float m = -1e30f; for (int k = 0; k < 64; ++k) m = fmaxf(m, sc[h][k]);
        float l = 0; for (int k = 0; k < 64; ++k) { float e = __expf(sc[h][k] - m); sc[h][k] = e; l += e; }
        pm[blockIdx.x * H + h] = m; pl[blockIdx.x * H + h] = l;
    }
    __syncthreads();
    for (int p = threadIdx.x; p < H * DC; p += 256) {
        int h = p / DC, c = p % DC; float a = 0;
        for (int k = 0; k < 64; ++k) a += sc[h][k] * bf2f(kv[k][c]);
        part[((size_t)blockIdx.x * H + h) * DC + c] = a;
    }
}
__global__ void k_comb(const float* part, const float* pm, const float* pl, const u16* w_uv, float* ctx) {
    __shared__ float lat[DC], wgt[256], red[16];
    int h = blockIdx.x, t = threadIdx.x;
    float m = -1e30f; for (int s = 0; s < 256; ++s) m = fmaxf(m, pm[s * H + h]);
    float l = 0; for (int s = 0; s < 256; ++s) l += pl[s * H + h] * __expf(pm[s * H + h] - m);
    if (t < 256) wgt[t] = __expf(pm[t * H + h] - m) / l;
    __syncthreads();
    float a = 0; for (int s = 0; s < 256; ++s) a += wgt[s] * part[((size_t)s * H + h) * DC + t];
    lat[t] = a;
    __syncthreads();
    if (t < D) { float b = 0; for (int c = 0; c < DC; ++c) b += lat[c] * bf2f(w_uv[((size_t)c * H + h) * D + t]); ctx[h * D + t] = rb(b); }
}
__global__ void k_o(const float* ctx, const u16* w_o, const u16* x, const float* mod, float* x2) {
    __shared__ float red[16];
    int m = blockIdx.x; float a = 0;
    for (int k = threadIdx.x; k < H * D; k += blockDim.x) a += ctx[k] * bf2f(w_o[(size_t)k * M + m]);
    a = block_sum(a, red);
    if (threadIdx.x == 0) x2[m] = rb(bf2f(x[m]) + rb(mod[2 * M + m] * rb(a)));
}
__global__ void k_gu(const float* x2, const float* mod, const u16* wg, const u16* wu, float* g) {
    __shared__ float xs[M], h[M], red[16];
    for (int i = threadIdx.x; i < M; i += blockDim.x) xs[i] = x2[i];
    __syncthreads();
    ln_mod(xs, mod + 3 * M, mod + 4 * M, h, red);
    int n = blockIdx.x * blockDim.x + threadIdx.x;
    float a = 0, b = 0; for (int k = 0; k < M; ++k) { a += h[k] * bf2f(wg[(size_t)k * F + n]); b += h[k] * bf2f(wu[(size_t)k * F + n]); }
    a = rb(a); b = rb(b);
    float t = tanhf(0.7978845608028654f * (a + 0.044715f * a * a * a));
    float ge = rb(0.5f * a * (1.f + t));
    g[n] = rb(ge * b);
}
__global__ void k_down(const float* g, const u16* wd, const float* x2, const float* mod, u16* out) {
    __shared__ float red[16];
    int m = blockIdx.x; float a = 0;
    for (int k = threadIdx.x; k < F; k += blockDim.x) a += g[k] * bf2f(wd[(size_t)k * M + m]);
    a = block_sum(a, red);
    if (threadIdx.x == 0) out[m] = f2bf(x2[m] + rb(mod[5 * M + m] * rb(a)));
}

extern "C" void kernel_launch(void* const* d_in, const int* in_sizes, int n_in, void* d_out, int out_size, void* d_ws, size_t ws_size, hipStream_t stream) {
    const u16* x = (const u16*)d_in[0]; const u16* ckv = (const u16*)d_in[1]; const u16* w_q = (const u16*)d_in[2];
    const u16* w_uk = (const u16*)d_in[3]; const u16* w_uv = (const u16*)d_in[4]; const u16* w_o = (const u16*)d_in[5];
    const u16* wg = (const u16*)d_in[7]; const u16* wu = (const u16*)d_in[8]; const u16* wd = (const u16*)d_in[9];
    const u16* c = (const u16*)d_in[10]; const u16* w_mod = (const u16*)d_in[11];
    float* ws = (float*)d_ws;
    float* mod = ws; float* q = mod + 4096; float* qa = q + 4096; float* pm = qa + H * DC; float* pl = pm + 256 * H;
    float* ctx = pl + 256 * H; float* x2 = ctx + 4096; float* g = x2 + 1024; float* part = g + 8192;
    k_mod<<<(6 * M + 63) / 64, 64, 0, stream>>>(c, w_mod, mod);
    k_q<<<4096 / 64, 64, 0, stream>>>(x, mod, w_q, q);
    k_qabs<<<DC * H / 4, 256, 0, stream>>>(q, w_uk, qa);
    k_attn<<<256, 256, 0, stream>>>(qa, ckv, part, pm, pl);
    k_comb<<<H, 512, 0, stream>>>(part, pm, pl, w_uv, ctx);
    k_o<<<M, 256, 0, stream>>>(ctx, w_o, x, mod, x2);
    k_gu<<<F / 64, 64, 0, stream>>>(x2, mod, wg, wu, g);
    k_down<<<M, 256, 0, stream>>>(g, wd, x2, mod, (u16*)d_out);
}
```

```cpp
#include <hip/hip_runtime.h>
#include <hip/hip_bf16.h>

#define STAMPS 0
#define LATE_DELAY 400
#define A_DELAY 0
#define QWAIT 2

typedef unsigned short u16;
typedef unsigned long long u64;
typedef unsigned v4u __attribute__((ext_vector_type(4)));
typedef unsigned v2u __attribute__((ext_vector_type(2)));
typedef float v4f __attribute__((ext_vector_type(4)));
typedef float v2f __attribute__((ext_vector_type(2)));
typedef float v16f __attribute__((ext_vector_type(16)));
typedef short v4s __attribute__((ext_vector_type(4)));
typedef unsigned short v2us __attribute__((ext_vector_type(2)));
typedef __bf16 v2bf __attribute__((ext_vector_type(2)));
typedef __bf16 v8bf __attribute__((ext_vector_type(8)));
#define LAS __attribute__((address_space(3)))
#define GAS __attribute__((address_space(1)))
typedef __amdgpu_buffer_rsrc_t rsrc_t;

constexpr int M = 576, DC = 512, SK = 16384, FF = 4608;
constexpr int NT = 512;
constexpr int CODE_BYTES = 24320;
constexpr int SPIN_MAX = 1 << 16;
constexpr unsigned POISON = 0xFFFFFFFFu;
constexpr int OOB = 0x40000000;
constexpr int WS_EPOCH = 0, WS_F1 = 256;
constexpr int WS_CL = 4096;
constexpr int WS_H1 = WS_CL + 128 * 128;
constexpr int WS_HQ = WS_H1 + 4 * 288 * 16;
constexpr int WS_ST = WS_HQ + 32 * 4 * 512;
constexpr int WS_QA = 256 * 1024;
constexpr int WS_PZ = WS_QA + 32768;
constexpr int WS_CTX = WS_PZ;
constexpr int WS_U5 = WS_CTX + 65536;
constexpr int WS_T5 = WS_U5 + 4608;
constexpr int WS_MRB = WS_T5 + 9216;
constexpr int WS_H6 = WS_MRB + 4608;
constexpr int WS_INB = 1 << 20;
constexpr int WS_STAMP = WS_INB + (4 << 20);
constexpr int WS_BYTES = WS_STAMP + 256 * 8 * 16 * 4;
static_assert(WS_ST + 32768 <= WS_QA && WS_H6 + 72 * 2304 <= WS_PZ + 8 * 32768 && WS_PZ + 8 * 32768 <= WS_INB, "ws map");
static_assert(WS_H1 % 128 == 0 && WS_HQ % 128 == 0 && WS_ST % 128 == 0 && WS_U5 % 128 == 0 && WS_T5 % 128 == 0 && WS_MRB % 128 == 0 && WS_H6 % 128 == 0, "line aligned");

#define RSRC(p, bytes) __builtin_amdgcn_make_buffer_rsrc((void*)(p), 0, (bytes), 0x00020000)
#define AUX_NT 2
#define AUX_SC1 16
__device__ __forceinline__ v4u ld_nt(rsrc_t r, int off) { return __builtin_amdgcn_raw_buffer_load_b128(r, off, 0, AUX_NT); }
__device__ __forceinline__ v2u ld64_nt(rsrc_t r, int off, int so) { return __builtin_amdgcn_raw_buffer_load_b64(r, off, so, AUX_NT); }
__device__ __forceinline__ v4u ld_sc1(rsrc_t r, int off) { return __builtin_amdgcn_raw_buffer_load_b128(r, off, 0, AUX_SC1); }
__device__ __forceinline__ unsigned ld32_sc1(rsrc_t r, int off) { return __builtin_amdgcn_raw_buffer_load_b32(r, off, 0, AUX_SC1); }
__device__ __forceinline__ void st_sc1(v4u v, rsrc_t r, int off) { __builtin_amdgcn_raw_buffer_store_b128(v, r, off, 0, AUX_SC1); }
__device__ __forceinline__ void st64_sc1(v2u v, rsrc_t r, int off) { __builtin_amdgcn_raw_buffer_store_b64(v, r, off, 0, AUX_SC1); }
__device__ __forceinline__ void st32_sc1(unsigned v, rsrc_t r, int off) { __builtin_amdgcn_raw_buffer_store_b32(v, r, off, 0, AUX_SC1); }
#define MEMBAR() asm volatile("" ::: "memory")
#define DRAIN() asm volatile("s_waitcnt vmcnt(0)" ::: "memory")
#define SCHED() __builtin_amdgcn_sched_barrier(0)
#define PIN(x) asm volatile("" : "+v"(x))
#define BAR() do { asm volatile("s_waitcnt lgkmcnt(0)\n\ts_barrier" ::: "memory"); } while (0)
#define RAWBAR() do { asm volatile("s_barrier" ::: "memory"); } while (0)

__device__ __forceinline__ float u2f(unsigned u) { return __builtin_bit_cast(float, u); }
__device__ __forceinline__ unsigned f2u(float f) { return __builtin_bit_cast(unsigned, f); }
__device__ __forceinline__ float bflo(unsigned u) { return u2f(u << 16); }
__device__ __forceinline__ float bfhi(unsigned u) { return u2f(u & 0xFFFF0000u); }
__device__ __forceinline__ unsigned pkbf(float lo, float hi) { v2f p = {lo, hi}; v2bf q = __builtin_convertvector(p, v2bf); return __builtin_bit_cast(unsigned, q); }
__device__ __forceinline__ float rb(float f) { return bflo(pkbf(f, f)); }
__device__ __forceinline__ float dot2(unsigned a, unsigned b, float c) { return __builtin_amdgcn_fdot2_f32_bf16(__builtin_bit_cast(v2bf, a), __builtin_bit_cast(v2bf, b), c, false); }
__device__ __forceinline__ unsigned perm(unsigned hi, unsigned lo, unsigned sel) { return __builtin_amdgcn_perm(hi, lo, sel); }
__device__ __forceinline__ unsigned umax(unsigned a, unsigned b) { return a > b ? a : b; }
__device__ __forceinline__ v4f mfma16(v4u a, v4u b, v4f c) { return __builtin_amdgcn_mfma_f32_16x16x32_bf16(__builtin_bit_cast(v8bf, a), __builtin_bit_cast(v8bf, b), c, 0, 0, 0); }
__device__ __forceinline__ v16f mfma32(v4u a, v4u b, v16f c) { return __builtin_amdgcn_mfma_f32_32x32x16_bf16(__builtin_bit_cast(v8bf, a), __builtin_bit_cast(v8bf, b), c, 0, 0, 0); }
__device__ __forceinline__ float sum4(v4f a) { return (a.x + a.z) + (a.y + a.w); }
__device__ __forceinline__ v4u pack4(float a, float b, float c, float d, unsigned tag) {
    const unsigned ud = f2u(d);
    v4u g; g.x = perm(f2u(a), ud, 0x07060501u); g.y = perm(f2u(b), ud, 0x07060502u); g.z = perm(f2u(c), ud, 0x07060503u); g.w = tag; return g;
}
__device__ __forceinline__ v4f unpack4(v4u g) {
    const unsigned gx = g.x, gy = g.y, gz = g.z;
    const unsigned t = perm(gy, gx, 0x0c04000cu);
    const unsigned d = perm(gz, t, 0x04020100u);
    v4f r = {u2f(gx & 0xFFFFFF00u), u2f(gy & 0xFFFFFF00u), u2f(gz & 0xFFFFFF00u), u2f(d)}; return r;
}
__device__ __forceinline__ float red32(float a, float b) { auto r = __builtin_amdgcn_permlane32_swap(f2u(a), f2u(b), false, false); const unsigned x = r[0], y = r[1]; return u2f(x) + u2f(y); }
__device__ __forceinline__ float red16(float a, float b) { auto r = __builtin_amdgcn_permlane16_swap(f2u(a), f2u(b), false, false); const unsigned x = r[0], y = r[1]; return u2f(x) + u2f(y); }
__device__ __forceinline__ float max32(float a, float b) { auto r = __builtin_amdgcn_permlane32_swap(f2u(a), f2u(b), false, false); const unsigned x = r[0], y = r[1]; return fmaxf(u2f(x), u2f(y)); }
__device__ __forceinline__ float max16(float a) { auto r = __builtin_amdgcn_permlane16_swap(f2u(a), f2u(a), false, false); const unsigned x = r[0], y = r[1]; return fmaxf(u2f(x), u2f(y)); }
template <int CTRL> __device__ __forceinline__ float dpp(float v) { return u2f((unsigned)__builtin_amdgcn_update_dpp(0, (int)f2u(v), CTRL, 0xf, 0xf, true)); }
__device__ __forceinline__ float add_x8(float v) { return v + dpp<0x128>(v); }
__device__ __forceinline__ float add_r4(float v) { return v + dpp<0x124>(v); }
__device__ __forceinline__ float add_x2(float v) { return v + dpp<0x4E>(v); }
__device__ __forceinline__ float add_x1(float v) { return v + dpp<0xB1>(v); }
__device__ __forceinline__ float row_sum(float v) { return add_r4(add_x8(add_x2(add_x1(v)))); }
__device__ __forceinline__ float row_max(float v) { v = fmaxf(v, dpp<0xB1>(v)); v = fmaxf(v, dpp<0x4E>(v)); v = fmaxf(v, dpp<0x128>(v)); return fmaxf(v, dpp<0x124>(v)); }
__device__ __forceinline__ float wave_sum(float v) { v = row_sum(v); v = red16(v, v); return red32(v, v); }
__device__ __forceinline__ void interleave(v4u& a, v4u& b) {
    const unsigned a0 = a.x, a1 = a.y, a2 = a.z, a3 = a.w, b0 = b.x, b1 = b.y, b2 = b.z, b3 = b.w;
    a.x = perm(b0, a0, 0x05040100u); a.y = perm(b0, a0, 0x07060302u); a.z = perm(b1, a1, 0x05040100u); a.w = perm(b1, a1, 0x07060302u);
    b.x = perm(b2, a2, 0x05040100u); b.y = perm(b2, a2, 0x07060302u); b.z = perm(b3, a3, 0x05040100u); b.w = perm(b3, a3, 0x07060302u);
}
__device__ __forceinline__ void frag4(const v2u* r, v4u* f) {
    unsigned o[4][4];
#pragma unroll
    for (int p = 0; p < 4; ++p) {
        const unsigned ax = r[2 * p].x, ay = r[2 * p].y, bx = r[2 * p + 1].x, by = r[2 * p + 1].y;
        o[0][p] = perm(bx, ax, 0x05040100u); o[1][p] = perm(bx, ax, 0x07060302u); o[2][p] = perm(by, ay, 0x05040100u); o[3][p] = perm(by, ay, 0x07060302u);
    }
#pragma unroll
    for (int n = 0; n < 4; ++n) { f[n] = v4u{o[n][0], o[n][1], o[n][2], o[n][3]}; PIN(f[n]); }
}
__device__ __forceinline__ void dot8(float (&acc)[8], const v4u& a, const v4u& b, unsigned xp) {
    const unsigned a0 = a.x, a1 = a.y, a2 = a.z, a3 = a.w, b0 = b.x, b1 = b.y, b2 = b.z, b3 = b.w;
    acc[0] = dot2(a0, xp, acc[0]); acc[1] = dot2(a1, xp, acc[1]); acc[2] = dot2(a2, xp, acc[2]); acc[3] = dot2(a3, xp, acc[3]);
    acc[4] = dot2(b0, xp, acc[4]); acc[5] = dot2(b1, xp, acc[5]); acc[6] = dot2(b2, xp, acc[6]); acc[7] = dot2(b3, xp, acc[7]);
}
__device__ __forceinline__ void dot8z(float (&acc)[8], const v4u& a, unsigned slo, unsigned shi) {
    const unsigned a0 = a.x, a1 = a.y, a2 = a.z, a3 = a.w;
    acc[0] = dot2(a0, slo, acc[0]); acc[1] = dot2(a0, shi, acc[1]); acc[2] = dot2(a1, slo, acc[2]); acc[3] = dot2(a1, shi, acc[3]);
    acc[4] = dot2(a2, slo, acc[4]); acc[5] = dot2(a2, shi, acc[5]); acc[6] = dot2(a3, slo, acc[6]); acc[7] = dot2(a3, shi, acc[7]);
}
#define ZERO8(acc) float acc[8] = {0.f, 0.f, 0.f, 0.f, 0.f, 0.f, 0.f, 0.f}; asm volatile("" : "+v"(acc[0]), "+v"(acc[1]), "+v"(acc[2]), "+v"(acc[3]), "+v"(acc[4]), "+v"(acc[5]), "+v"(acc[6]), "+v"(acc[7]))
__device__ __forceinline__ float red8cols(const float (&a)[8], int lane) {
    const float b0 = red32(a[0], a[4]), b1 = red32(a[1], a[5]), b2 = red32(a[2], a[6]), b3 = red32(a[3], a[7]);
    const float c0 = red16(b0, b2), c1 = red16(b1, b3);
    const float d0 = add_x8(c0), d1 = add_x8(c1);
    return (lane & 8) ? d1 : d0;
}
__device__ __forceinline__ float silu_bf(float v) { return rb(v * __builtin_amdgcn_rcpf(1.f + __expf(-v))); }
__device__ __forceinline__ u64 rt() { return __builtin_amdgcn_s_memrealtime(); }
#define STAMP(i)
#define STAMP_FLUSH()
#define CYC0()
#define CYC1()

__global__ void __launch_bounds__(NT, 2) fused_kernel(const u16* __restrict__ x, const u16* __restrict__ ckv, const u16* __restrict__ wq, const u16* __restrict__ wuk,
                                                      const u16* __restrict__ wuv, const u16* __restrict__ wo, const u16* __restrict__ wg, const u16* __restrict__ wu,
                                                      const u16* __restrict__ wd, const u16* __restrict__ cc, const u16* __restrict__ wmod, u16* __restrict__ out,
                                                      unsigned char* __restrict__ ws) {
    __shared__ __attribute__((aligned(16))) unsigned char smem[163840];
    LAS unsigned char* L = (LAS unsigned char*)smem;
    u64 pc0; asm volatile("s_getpc_b64 %0" : "=s"(pc0));
    const u64 t_start = rt();
    asm volatile("" : "+s"(x), "+s"(ckv), "+s"(wq), "+s"(wuk), "+s"(wuv), "+s"(wo), "+s"(wg), "+s"(wu), "+s"(wd), "+s"(cc), "+s"(wmod), "+s"(out), "+s"(ws));
    const int bid = blockIdx.x, tid = threadIdx.x, lane = tid & 63, w = __builtin_amdgcn_readfirstlane(tid >> 6);
    const rsrc_t rws = RSRC(ws, WS_BYTES);
    const rsrc_t rx = RSRC(x, M * 2);
    const int l15 = lane & 15, kq = lane >> 4, half = lane >> 5;

    if (__builtin_expect(bid < 128, 1)) {
        const int b = bid, hh = b >> 2, mj = b & 3;
        const rsrc_t rmod = RSRC(wmod, M * 6 * M * 2), rc = RSRC(cc, M * 2);
        LAS float* red = (LAS float*)L;
        LAS unsigned* hp = (LAS unsigned*)(L + 20480);
        const bool doS1 = b < 72;
        unsigned tag;
        if (__builtin_expect(w < 4, 1)) {
            const int kp = b / 18, nt = b - 18 * kp;
            const int r1 = 144 * kp + 36 * w + (lane >> 3);
            const int co = doS1 ? r1 * 2 : OOB, wo1 = doS1 ? r1 * 6912 + nt * 128 + (lane & 7) * 16 : OOB;
            unsigned s1c[5]; v4u s1w[5], q2[9];
#pragma unroll
            for (int i = 0; i < 5; ++i) s1c[i] = __builtin_amdgcn_raw_buffer_load_b16(rc, (i < 4 || lane < 32) ? co + 16 * i : OOB, 0, 0);
#pragma unroll
            for (int i = 0; i < 5; ++i) s1w[i] = ld_nt(rmod, (i < 4 || lane < 32) ? wo1 + 8 * i * 6912 : OOB);
            tag = ld32_sc1(rws, WS_EPOCH);
            const rsrc_t rq = RSRC(wq, M * 8192);
            const int qo = (144 * mj + 36 * w) * 8192 + hh * 256 + l15 * 16;
#pragma unroll
            for (int i = 0; i < 8; ++i) q2[i] = ld_nt(rq, qo + (8 * (i >> 1) + 2 * kq + (i & 1)) * 8192);
            q2[8] = ld_nt(rq, qo + (32 + kq) * 8192);
            SCHED();
            STAMP(9);
            if (doS1) {
                unsigned slo[5], shi[5];
#pragma unroll
                for (int i = 0; i < 5; ++i) { shi[i] = f2u(silu_bf(bflo(s1c[i]))); slo[i] = shi[i] >> 16; PIN(shi[i]); PIN(slo[i]); }
                ZERO8(acc);
                SCHED();
#pragma unroll
                for (int i = 0; i < 5; ++i) dot8z(acc, s1w[i], slo[i], shi[i]);
                red[w * 64 + (lane & 7) * 8 + (lane >> 3)] = red8cols(acc, lane);
                STAMP(10);
                BAR();
                if (tid < 16) {
                    const v4f s = (*(LAS v4f*)(red + tid * 4) + *(LAS v4f*)(red + 64 + tid * 4)) + (*(LAS v4f*)(red + 128 + tid * 4) + *(LAS v4f*)(red + 192 + tid * 4));
                    st_sc1(pack4(s.x, s.y, s.z, s.w, tag + 1u), rws, WS_H1 + kp * 4608 + (nt * 16 + tid) * 16);
                }
                STAMP(1);
                BAR();
            }
            PIN(tag); tag += 1u;
#pragma unroll
            for (int i = 0; i < 4; ++i) { interleave(q2[2 * i], q2[2 * i + 1]); PIN(q2[2 * i]); PIN(q2[2 * i + 1]); }
            PIN(q2[8]);
            ZERO8(acc);
            STAMP(12);
            BAR();
            STAMP(2);
            {
                const unsigned h0 = hp[18 * w + kq], h1 = hp[18 * w + 4 + kq], h2 = hp[18 * w + 8 + kq], h3 = hp[18 * w + 12 + kq];
                const unsigned hs = ((LAS u16*)hp)[36 * w + 32 + kq];
                dot8(acc, q2[0], q2[1], h0); dot8(acc, q2[2], q2[3], h1); dot8(acc, q2[4], q2[5], h2); dot8(acc, q2[6], q2[7], h3);
                dot8z(acc, q2[8], hs, hs << 16);
                const float b0 = red32(acc[0], acc[4]), b1 = red32(acc[1], acc[5]), b2 = red32(acc[2], acc[6]), b3 = red32(acc[3], acc[7]);
                v2f c; c.x = red16(b0, b2); c.y = red16(b1, b3);
                *(LAS v2f*)(red + w * 128 + l15 * 8 + kq * 2) = c;
            }
            BAR();
            if (tid < 32) {
                const v4f s = (*(LAS v4f*)(red + tid * 4) + *(LAS v4f*)(red + 128 + tid * 4)) + (*(LAS v4f*)(red + 256 + tid * 4) + *(LAS v4f*)(red + 384 + tid * 4));
                st_sc1(pack4(s.x, s.y, s.z, s.w, tag), rws, WS_HQ + b * 512 + tid * 16);
            }
            STAMP(3);
            if (w == 1) {
                const int of = lane == 0 ? WS_F1 : (lane < 9 ? WS_CL + lane * 128 : WS_CL + b * 128);
                for (int sp = 0;; ++sp) {
                    MEMBAR();
                    const unsigned f = ld32_sc1(rws, of);
                    if (__all(f == tag) || sp > SPIN_MAX) break;
                }
            }
            BAR();
            if (tid < 16) st_sc1(*(LAS v4u*)(L + 24576 + tid * 16), rws, WS_QA + hh * 1024 + mj * 256 + tid * 16);
            STAMP(5);
        } else {
            tag = ld32_sc1(rws, WS_EPOCH);
            float npair0 = 0.f, npair1 = 0.f;
            const int j = (lane & 31) < 18 ? (lane & 31) : 17;
            if (w < 6) {
                const v4u xa = __builtin_amdgcn_raw_buffer_load_b128(rx, lane * 16, 0, 0);
                const unsigned xb = __builtin_amdgcn_raw_buffer_load_b32(rx, 1024 + lane * 4, 0, 0);
                const int m0 = 144 * mj + 4 * (18 * (w - 4) + j) + 2 * half;
                const unsigned xm = __builtin_amdgcn_raw_buffer_load_b32(rx, m0 * 2, 0, 0);
                if (doS1) { BAR(); BAR(); }
                const unsigned a0 = xa.x, a1 = xa.y, a2 = xa.z, a3 = xa.w;
                float v[10] = {bflo(a0), bfhi(a0), bflo(a1), bfhi(a1), bflo(a2), bfhi(a2), bflo(a3), bfhi(a3), bflo(xb), bfhi(xb)};
                float s = 0.f;
#pragma unroll
                for (int i = 0; i < 10; ++i) s += v[i];
                const float mean = wave_sum(s) * (1.f / M);
                float q = 0.f;
#pragma unroll
                for (int i = 0; i < 8; ++i) q += (v[i] - mean) * (v[i] - mean);
                if (lane < 32) q += (v[8] - mean) * (v[8] - mean) + (v[9] - mean) * (v[9] - mean);
                const float var = rb(wave_sum(q) * (1.f / M));
                const float rden = __builtin_amdgcn_rcpf(rb(__builtin_amdgcn_sqrtf(rb(var + 1e-5f)))), mb = rb(mean);
                npair0 = rb(rb(bflo(xm) - mb) * rden); npair1 = rb(rb(bfhi(xm) - mb) * rden);
            } else if (doS1) { BAR(); BAR(); }
            v4u k3[8];
            {
                const rsrc_t rk = RSRC(wuk, DC * 8192);
                const int ko = (128 * mj + 32 * (w - 4) + kq) * 8192 + hh * 256 + l15 * 16;
#pragma unroll
                for (int i = 0; i < 8; ++i) k3[i] = ld_nt(rk, ko + 4 * i * 8192);
                SCHED();
            }
            PIN(tag); tag += 1u;
            PIN(npair0); PIN(npair1);
            STAMP(11);
            if (w < 6) {
                const int cg = 36 * mj + 18 * (w - 4) + j;
                const int o = WS_H1 + (2 * half) * 4608 + cg * 16;
                v4u g0, g1, g2, g3;
                for (int sp = 0;; ++sp) {
                    MEMBAR();
                    g0 = ld_sc1(rws, o); g1 = ld_sc1(rws, o + 4608); g2 = ld_sc1(rws, o + 2304); g3 = ld_sc1(rws, o + 2304 + 4608);
                    const bool ok = g0.w == tag && g1.w == tag && g2.w == tag && g3.w == tag;
                    if (__all(ok) || sp > SPIN_MAX) break;
                }
                STAMP(12);
                const v4f sa = unpack4(g0) + unpack4(g1), ca = unpack4(g2) + unpack4(g3);
                const unsigned sp = pkbf(red32(sa.x, sa.z), red32(sa.y, sa.w)), cp = pkbf(red32(ca.x, ca.z), red32(ca.y, ca.w));
                const unsigned c1 = pkbf(1.f + bflo(cp), 1.f + bfhi(cp));
                const unsigned t1 = pkbf(npair0 * bflo(c1), npair1 * bfhi(c1));
                hp[2 * (18 * (w - 4) + j) + half] = pkbf(bflo(t1) + bflo(sp), bfhi(t1) + bfhi(sp));
            }
            BAR();
            BAR();
            {
                const int o = WS_HQ + (hh * 4 + kq) * 512 + l15 * 32;
                v4u g0, g1;
                for (int sp = 0;; ++sp) {
                    MEMBAR();
                    g0 = ld_sc1(rws, o); g1 = ld_sc1(rws, o + 16);
                    if (__all(g0.w == tag && g1.w == tag) || sp > SPIN_MAX) break;
                }
                STAMP(4);
                const v4f qa = unpack4(g0), qb = unpack4(g1);
                const float b0 = red32(qa.x, qb.x), b1 = red32(qa.y, qb.y), b2 = red32(qa.z, qb.z), b3 = red32(qa.w, qb.w);
                const unsigned pp = pkbf(red16(b0, b2), red16(b1, b3));
                auto s16 = __builtin_amdgcn_permlane16_swap(pp, pp, false, false);
                const unsigned pe = s16[0], po = s16[1];
                auto se = __builtin_amdgcn_permlane32_swap(pe, pe, false, false);
                auto so = __builtin_amdgcn_permlane32_swap(po, po, false, false);
                const unsigned p0 = se[0], p2 = se[1], p1 = so[0], p3 = so[1];
                float a[8];
#pragma unroll
                for (int i = 0; i < 8; ++i) { const unsigned k0 = k3[i].x, k1 = k3[i].y, k2 = k3[i].z, k3w = k3[i].w; a[i] = dot2(k3w, p3, dot2(k2, p2, dot2(k1, p1, dot2(k0, p0, 0.f)))); }
                float c[4];
#pragma unroll
                for (int i = 0; i < 4; ++i) { const float u0 = add_x8(a[i]), u1 = add_x8(a[i + 4]); c[i] = (lane & 8) ? u1 : u0; }
                const float e0 = add_x2(c[0]), e1 = add_x2(c[1]), e2 = add_x2(c[2]), e3 = add_x2(c[3]);
                const float f0 = (lane & 2) ? e2 : e0, f1 = (lane & 2) ? e3 : e1;
                const float g0f = add_x1(f0), g1f = add_x1(f1);
                float r = (lane & 1) ? g1f : g0f;
                r += dpp<0x141>(dpp<0x1B>(r));
                r *= 0.08838834764831845f * 1.4426950408889634f;
                LAS u16* qo = (LAS u16*)(L + 24576);
                const int i = ((lane >> 1) & 4) | (lane & 3);
                qo[32 * (w - 4) + 4 * i + kq] = (u16)pkbf(r, r);
            }
            BAR();
            STAMP(5);
        }
        if (LATE_DELAY) { while (rt() - t_start < (u64)LATE_DELAY) __builtin_amdgcn_s_sleep(1); }
        if (b >= 56) {
            const int f = b - 56;
            const int ks0 = 9 * (w & 1) + (w < 4 ? 0 : 5);
            v4u gf[5][4], df[2][4], ef[2][4];
            {
                const rsrc_t rg = RSRC((w & 2) ? wu : wg, M * FF * 2), rd = RSRC(wd, M * FF * 2);
                const int go = (32 * ks0 + 8 * kq) * 9216 + f * 128 + l15 * 8;
                const int dofs = (64 * f + 8 * kq) * 1152 + l15 * 8;
                v2u gr[5][8], dr[2][8], er[2][8];
#pragma unroll
                for (int u = 0; u < 4; ++u)
#pragma unroll
                    for (int e = 0; e < 8; ++e) gr[u][e] = ld64_nt(rg, go, (32 * u + e) * 9216);
#pragma unroll
                for (int u = 0; u < 2; ++u)
#pragma unroll
                    for (int e = 0; e < 8; ++e) dr[u][e] = ld64_nt(rd, dofs, (32 * u + e) * 1152 + w * 128);
                if (w < 4) {
#pragma unroll
                    for (int e = 0; e < 8; ++e) gr[4][e] = ld64_nt(rg, go, (128 + e) * 9216);
                    SCHED();
                    frag4(gr[4], gf[4]);
                } else if (w == 5) {
#pragma unroll
                    for (int u = 0; u < 2; ++u)
#pragma unroll
                        for (int e = 0; e < 8; ++e) er[u][e] = ld64_nt(rd, dofs, (32 * u + e) * 1152 + 1024);
                    SCHED();
                    frag4(er[0], ef[0]); frag4(er[1], ef[1]);
                }
                SCHED();
#pragma unroll
                for (int u = 0; u < 4; ++u) frag4(gr[u], gf[u]);
                frag4(dr[0], df[0]); frag4(dr[1], df[1]);
            }
            STAMP(6);
            LAS float* P = (LAS float*)L;
            LAS float* Sx = (LAS float*)(L + 8192);
            float a1g, a1u, a2g, a2u;
            {
                const int ao = (l15 == 0 ? WS_MRB + 2 * 1152 : (l15 == 4 ? WS_MRB + 1152 : OOB)) + (32 * ks0 + 8 * kq) * 2;
                v4u a[5];
                for (int sp = 0;; ++sp) {
                    MEMBAR();
#pragma unroll
                    for (int u = 0; u < 5; ++u) a[u] = ld_sc1(rws, (u < 4 || w < 4) ? ao + 64 * u : OOB);
                    unsigned mx = 0;
#pragma unroll
                    for (int u = 0; u < 5; ++u) mx = umax(umax(mx, a[u].x), a[u].z);
                    if (__all(mx != POISON) || sp > SPIN_MAX) break;
                    __builtin_amdgcn_s_sleep(2);
                }
                v4f acc[4] = {};
#pragma unroll
                for (int u = 0; u < 4; ++u)
#pragma unroll
                    for (int n = 0; n < 4; ++n) acc[n] = mfma16(a[u], gf[u][n], acc[n]);
                if (w < 4) {
#pragma unroll
                    for (int n = 0; n < 4; ++n) acc[n] = mfma16(a[4], gf[4][n], acc[n]);
                }
                const v4f o = {acc[0].x, acc[1].x, acc[2].x, acc[3].x};
                if (lane < 32) *(LAS v4f*)(P + 512 + kq * 512 + w * 64 + l15 * 4) = o;
                BAR();
                LAS float* Q1 = P + 512 + lane; LAS float* Q2 = P + 1024 + lane;
                a1g = (Q1[0] + Q1[64]) + (Q1[256] + Q1[320]); a1u = (Q1[128] + Q1[192]) + (Q1[384] + Q1[448]);
                a2g = (Q2[0] + Q2[64]) + (Q2[256] + Q2[320]); a2u = (Q2[128] + Q2[192]) + (Q2[384] + Q2[448]);
                PIN(a1g); PIN(a1u); PIN(a2g); PIN(a2u);
            }
            STAMP(13);
            const int uo = (l15 < 4 ? WS_U5 + l15 * 1152 : OOB) + (32 * ks0 + 8 * kq) * 2;
            v4f acc[4] = {};
            float rs = 0.f, kg = 0.f, ku = 0.f;
            if (w < 4) {
                v4u a[5];
                for (int sp = 0;; ++sp) {
                    MEMBAR();
#pragma unroll
                    for (int u = 0; u < 5; ++u) a[u] = ld_sc1(rws, uo + 64 * u);
                    unsigned mx = 0;
#pragma unroll
                    for (int u = 0; u < 5; ++u) mx = umax(umax(umax(umax(mx, a[u].x), a[u].y), a[u].z), a[u].w);
                    if (__all(mx != POISON) || sp > SPIN_MAX) break;
                }
                STAMP(7);
#pragma unroll
                for (int u = 0; u < 5; ++u)
#pragma unroll
                    for (int n = 0; n < 4; ++n) acc[n] = mfma16(a[u], gf[u][n], acc[n]);
                SCHED();
                const v4f o = {sum4(acc[0]), sum4(acc[1]), sum4(acc[2]), sum4(acc[3])};
                if (lane < 16) *(LAS v4f*)(P + w * 64 + l15 * 4) = o;
                RAWBAR();
                if (w == 0) {
                    const float mean = sum4(*(LAS v4f*)Sx) * (1.f / M);
                    const float var = rb(fmaxf(sum4(*(LAS v4f*)(Sx + 4)) * (1.f / M) - mean * mean, 0.f));
                    rs = __builtin_amdgcn_rcpf(rb(__builtin_amdgcn_sqrtf(rb(var + 1e-5f))));
                    const float mr = rb(mean) * rs;
                    kg = a2g - mr * a1g; ku = a2u - mr * a1u;
                    PIN(rs); PIN(kg); PIN(ku);
                }
            } else {
                v4u a[4], t[3];
                const int to = WS_T5 + kq * 2304 + ((w - 4) * 2 * 16 + l15) * 16;
                const int to2 = w == 7 ? WS_T5 + kq * 2304 + (128 + l15) * 16 : OOB;
                for (int sp = 0;; ++sp) {
                    MEMBAR();
                    t[0] = ld_sc1(rws, to); t[1] = ld_sc1(rws, to + 256); t[2] = ld_sc1(rws, to2);
#pragma unroll
                    for (int u = 0; u < 4; ++u) a[u] = ld_sc1(rws, uo + 64 * u);
                    unsigned mx = 0;
#pragma unroll
                    for (int u = 0; u < 3; ++u) mx = umax(umax(mx, t[u].x), t[u].z);
#pragma unroll
                    for (int u = 0; u < 4; ++u) mx = umax(umax(umax(umax(mx, a[u].x), a[u].y), a[u].z), a[u].w);
                    if (__all(mx != POISON) || sp > SPIN_MAX) break;
                }
                STAMP(7);
                float s1 = 0.f, s2 = 0.f;
#pragma unroll
                for (int u = 0; u < 3; ++u) {
                    const unsigned t0 = t[u].x, t1 = t[u].y, t2 = t[u].z, t3 = t[u].w;
                    const float xv = red16(red32(u2f(t0), u2f(t2)), red32(u2f(t1), u2f(t3)));
                    s1 += xv; s2 += xv * xv;
                }
                float s = red32(s1, s2); s = red16(s, s); s = row_sum(s);
                if ((lane & 31) == 0) Sx[half * 4 + (w - 4)] = s;
                BAR();
#pragma unroll
                for (int u = 0; u < 4; ++u)
#pragma unroll
                    for (int n = 0; n < 4; ++n) acc[n] = mfma16(a[u], gf[u][n], acc[n]);
                const v4f o = {sum4(acc[0]), sum4(acc[1]), sum4(acc[2]), sum4(acc[3])};
                if (lane < 16) *(LAS v4f*)(P + w * 64 + l15 * 4) = o;
            }
            BAR();
            LAS u16* gs = (LAS u16*)(L + 8448);
            if (w == 0) {
                LAS float* Q = P + lane;
                const float G = (Q[0] + Q[64]) + (Q[256] + Q[320]), U = (Q[128] + Q[192]) + (Q[384] + Q[448]);
                const unsigned gu = pkbf(rs * G + kg, rs * U + ku);
                const float ag = bflo(gu), au = bfhi(gu);
                constexpr float K0 = -2.f * 1.4426950408889634f * 0.7978845608028654f, K1 = K0 * 0.044715f;
                const float e = __builtin_amdgcn_exp2f(ag * (K0 + K1 * ag * ag));
                const float ge = rb(ag * __builtin_amdgcn_rcpf(1.f + e));
                gs[lane] = (u16)pkbf(ge * au, 0.f);
            }
            BAR();
            STAMP(14);
            {
                const v4u g0 = *(LAS v4u*)(gs + 8 * kq), g1 = *(LAS v4u*)(gs + 32 + 8 * kq);
                v4f y[4] = {};
#pragma unroll
                for (int n = 0; n < 4; ++n) y[n] = mfma16(g0, df[0][n], y[n]);
#pragma unroll
                for (int n = 0; n < 4; ++n) y[n] = mfma16(g1, df[1][n], y[n]);
                const v4u o = {f2u(y[0].x), f2u(y[1].x), f2u(y[2].x), f2u(y[3].x)};
                if (lane < 16) st_sc1(o, rws, WS_H6 + f * 2304 + w * 256 + l15 * 16);
                if (w == 5) {
                    v4f z[4] = {};
#pragma unroll
                    for (int n = 0; n < 4; ++n) z[n] = mfma16(g0, ef[0][n], z[n]);
#pragma unroll
                    for (int n = 0; n < 4; ++n) z[n] = mfma16(g1, ef[1][n], z[n]);
                    const v4u o2 = {f2u(z[0].x), f2u(z[1].x), f2u(z[2].x), f2u(z[3].x)};
                    if (lane < 16) st_sc1(o2, rws, WS_H6 + f * 2304 + 2048 + l15 * 16);
                }
            }
            STAMP(8);
        } else if (b >= 20) {
            const int o6 = b - 20, kg = o6 / 9, nt = o6 - 9 * kg;
            const int head = 8 * kg + w;
            v4u of[4][4];
            {
                const rsrc_t ro = RSRC(wo, 4096 * M * 2);
                const int oo = (128 * head + 8 * kq) * 1152 + nt * 128 + l15 * 8;
                v2u orr[4][8];
#pragma unroll
                for (int u = 0; u < 4; ++u)
#pragma unroll
                    for (int e = 0; e < 8; ++e) orr[u][e] = ld64_nt(ro, oo, (32 * u + e) * 1152);
                SCHED();
#pragma unroll
                for (int u = 0; u < 4; ++u) frag4(orr[u], of[u]);
            }
            STAMP(6);
            float ga0 = 0.f, ga1 = 0.f, c10 = 0.f, c11 = 0.f, x0 = 0.f, x1 = 0.f;
            if (w == 0) {
                const int mo = (64 * nt + 2 * (lane & 31)) * 2;
                const unsigned xr = __builtin_amdgcn_raw_buffer_load_b32(rx, kg == 0 ? mo : OOB, 0, 0);
                unsigned g, c;
                for (int sp = 0;; ++sp) {
                    MEMBAR();
                    g = ld32_sc1(rws, WS_MRB + mo); c = ld32_sc1(rws, WS_MRB + 2 * 1152 + mo);
                    if (__all(g != POISON && c != POISON) || sp > SPIN_MAX) break;
                    __builtin_amdgcn_s_sleep(2);
                }
                ga0 = bflo(g); ga1 = bfhi(g); c10 = bflo(c); c11 = bfhi(c); x0 = bflo(xr); x1 = bfhi(xr);
                PIN(ga0); PIN(ga1); PIN(c10); PIN(c11); PIN(x0); PIN(x1);
            }
            STAMP(13);
            const int co = (l15 < 8 ? WS_CTX + ((head >> 1) * 8 + l15) * 512 + (head & 1) * 256 : OOB) + kq * 16;
            v4u a[4];
            for (int sp = 0;; ++sp) {
                MEMBAR();
#pragma unroll
                for (int u = 0; u < 4; ++u) a[u] = ld_sc1(rws, co + 64 * u);
                unsigned mx = 0;
#pragma unroll
                for (int u = 0; u < 4; ++u) mx = umax(umax(mx, a[u].x), a[u].z);
                if (__all(mx != POISON) || sp > SPIN_MAX) break;
            }
            STAMP(7);
            v4f acc[4] = {};
#pragma unroll
            for (int u = 0; u < 4; ++u)
#pragma unroll
                for (int n = 0; n < 4; ++n) acc[n] = mfma16(a[u], of[u][n], acc[n]);
            v2f c; c.x = red16(sum4(acc[0]), sum4(acc[2])); c.y = red16(sum4(acc[1]), sum4(acc[3]));
            if (lane < 32) *(LAS v2f*)(red + w * 64 + l15 * 4 + kq * 2) = c;
            BAR();
            if (tid < 32) {
                LAS v2f* R = (LAS v2f*)red + lane;
                const v2f s = ((R[0] + R[32]) + (R[64] + R[96])) + ((R[128] + R[160]) + (R[192] + R[224]));
                const float t0 = x0 + ga0 * s.x, t1 = x1 + ga1 * s.y;
                st32_sc1(pkbf(t0 * c10, t1 * c11), rws, WS_U5 + kg * 1152 + nt * 128 + lane * 4);
                st64_sc1(v2u{f2u(t0), f2u(t1)}, rws, WS_T5 + kg * 2304 + nt * 256 + lane * 8);
            }
            STAMP(8);
        } else if (b < 18) {
            {
                v4u mw[18]; unsigned mc[18];
#pragma unroll
                for (int i = 0; i < 18; ++i) mc[i] = __builtin_amdgcn_raw_buffer_load_b16(rc, (72 * w + 4 * i + kq) * 2, 0, 0);
#pragma unroll
                for (int i = 0; i < 18; ++i) mw[i] = ld_nt(rmod, (72 * w + 4 * i + kq) * 6912 + 2304 + b * 256 + l15 * 16);
                SCHED();
#pragma unroll
                for (int i = 0; i < 18; ++i) { mc[i] = f2u(silu_bf(bflo(mc[i]))); PIN(mc[i]); }
                ZERO8(acc);
#pragma unroll
                for (int i = 0; i < 18; ++i) dot8z(acc, mw[i], mc[i] >> 16, mc[i]);
                const float b0 = red32(acc[0], acc[4]), b1 = red32(acc[1], acc[5]), b2 = red32(acc[2], acc[6]), b3 = red32(acc[3], acc[7]);
                v2f c; c.x = red16(b0, b2); c.y = red16(b1, b3);
                *(LAS v2f*)(red + w * 128 + l15 * 8 + kq * 2) = c;
                BAR();
                if (tid < 32) {
                    v4f s = *(LAS v4f*)(red + tid * 4);
#pragma unroll
                    for (int k = 1; k < 8; ++k) s += *(LAS v4f*)(red + k * 128 + tid * 4);
                    const int col = 128 * b + 4 * tid;
                    const float one = (col >= 1152 && col < 1728) ? 1.f : 0.f;
                    st64_sc1(v2u{pkbf(rb(s.x) + one, rb(s.y) + one), pkbf(rb(s.z) + one, rb(s.w) + one)}, rws, WS_MRB + col * 2);
                }
                STAMP(6);
            }
            if (w == 0) {
                const int cg = 8 * b + (lane & 7);
                const int m = 4 * cg + 2 * half + (kq & 1);
                v4u t; unsigned gb;
                for (int sp = 0;; ++sp) {
                    MEMBAR();
                    gb = ld32_sc1(rws, WS_MRB + 3 * 1152 + (m & ~1) * 2);
                    t = ld_sc1(rws, WS_T5 + kq * 2304 + cg * 16);
                    if (__all(umax(umax(t.x, t.z), gb) != POISON) || sp > SPIN_MAX) break;
                    __builtin_amdgcn_s_sleep(4);
                }
                const unsigned t0 = t.x, t1 = t.y, t2 = t.z, t3 = t.w;
                float x2 = rb(red16(red32(u2f(t0), u2f(t2)), red32(u2f(t1), u2f(t3))));
                float gsel = (kq & 1) ? bfhi(gb) : bflo(gb);
                PIN(x2); PIN(gsel);
                STAMP(7);
                v4f s;
                for (int sp = 0;; ++sp) {
                    MEMBAR();
                    v4u y[9];
#pragma unroll
                    for (int i = 0; i < 9; ++i) y[i] = ld_sc1(rws, WS_H6 + (8 * i + (lane >> 3)) * 2304 + cg * 16);
                    SCHED();
                    s = __builtin_bit_cast(v4f, y[0]);
#pragma unroll
                    for (int i = 1; i < 9; ++i) s += __builtin_bit_cast(v4f, y[i]);
                    if (__all(s.x == s.x) || sp > SPIN_MAX) break;
                }
                const float yv = rb(add_x8(red16(red32(s.x, s.z), red32(s.y, s.w))));
                const float o = x2 + rb(gsel * yv);
                if ((lane & 8) == 0) out[m] = (u16)pkbf(o, o);
                if (b == 0 && lane == 0) st32_sc1(tag, rws, WS_EPOCH);
                STAMP(8);
            }
        }
    } else {
        const int a = bid - 128;
        constexpr int QOFF = 131072;
        unsigned tag = ld32_sc1(rws, WS_EPOCH);
        {
            const v4u poison = {POISON, POISON, POISON, POISON};
            if (a < 9) {
#pragma unroll
                for (int i = 0; i < 4; ++i) st_sc1(poison, rws, WS_QA + a * 32768 + (i * NT + tid) * 16);
                if (a == 0) { DRAIN(); BAR(); if (tid == 0) st32_sc1(tag + 1u, rws, WS_F1); }
            }
#pragma unroll
            for (int i = 0; i < 4; ++i) st_sc1(poison, rws, WS_INB + a * 32768 + (i * NT + tid) * 16);
            if (w == 7) {
                const rsrc_t rcode = RSRC((const void*)(pc0 & ~255ull), CODE_BYTES);
                unsigned acc = 0;
#pragma unroll
                for (int i = 0; i < (CODE_BYTES + 8191) / 8192; ++i) acc ^= __builtin_amdgcn_raw_buffer_load_b32(rcode, (i * 64 + lane) * 128, 0, 0);
                asm volatile("" :: "v"(acc));
            }
            STAMP(9);
            DRAIN();
            STAMP(10);
            BAR();
            PIN(tag); tag += 1u;
            if (tid == 0) st32_sc1(tag, rws, WS_CL + a * 128);
            if (w == 0) {
                for (int sp = 0;; ++sp) {
                    MEMBAR();
                    const unsigned f = ld32_sc1(rws, WS_F1);
                    if (__all(f == tag) || sp > SPIN_MAX) break;
                    __builtin_amdgcn_s_sleep(2);
                }
                if (A_DELAY) { while (rt() - t_start < (u64)A_DELAY) __builtin_amdgcn_s_sleep(1); }
            }
            BAR();
        }
        STAMP(1);
        {
            const rsrc_t rkv = RSRC(ckv, SK * DC * 2);
            const int g0 = ((w & 3) << 2) | ((w >> 2) & 3), g1 = ((w & 3) << 2) | ((2 + (w >> 2)) & 3);
            const int vo0 = (lane ^ g0) * 16, vo1 = (lane ^ g1) * 16;
#pragma unroll
            for (int i = 0; i < 16; ++i)
                __builtin_amdgcn_raw_ptr_buffer_load_lds(rkv, (LAS void*)(L + (8 * i + w) * 1024), 16, (i & 1) ? vo1 : vo0, (a * 128 + 8 * i + w) * 1024, 0, AUX_NT);
            SCHED();
        }
        STAMP(2);
        const int hpair = a >> 3, cq = a & 7;
        unsigned ad[8];
        if (w < 4) {
            const int r = lane & 31, g = ((r & 3) << 2) | ((r >> 2) & 3);
#pragma unroll
            for (int j = 0; j < 8; ++j) { ad[j] = w * 32768 + r * 1024 + (((2 * j + half) ^ g) * 16); PIN(ad[j]); }
        } else {
            const int blk = kq & 1, q = l15 >> 2, p = lane & 3;
#pragma unroll
            for (int j = 0; j < 8; ++j) {
                const int t = j >> 1, tt = j & 1, g = (q << 2) | ((2 * tt + half) & 3);
                ad[j] = (8 * tt + 4 * half + q) * 1024 + (w - 4) * 256 + (((4 * t + 2 * blk + (p >> 1)) ^ g) * 16) + 8 * (p & 1); PIN(ad[j]);
            }
        }
        unsigned qw[4];
#pragma unroll
        for (int i = 0; i < 4; ++i) { qw[i] = QOFF + (4 * w + i) * 1024 + ((lane ^ ((i << 2) | (w & 3))) * 16); PIN(qw[i]); }
        asm volatile("s_waitcnt vmcnt(%0)" :: "i"(QWAIT) : "memory");
        STAMP(11);
        {
            v4u q[4];
            for (int sp = 0;; ++sp) {
                MEMBAR();
#pragma unroll
                for (int i = 0; i < 4; ++i) q[i] = ld_sc1(rws, WS_QA + (4 * w + i) * 1024 + lane * 16);
                if (__all(umax(umax(q[0].x, q[1].x), umax(q[2].x, q[3].x)) != POISON) || sp > SPIN_MAX) break;
            }
            DRAIN();
            STAMP(12);
#pragma unroll
            for (int i = 0; i < 4; ++i) *(LAS v4u*)(L + qw[i]) = q[i];
        }
        LAS float* mx = (LAS float*)(L + QOFF + 8192);
        LAS float* ls = (LAS float*)(L + QOFF + 8704);
        if (w < 4) {
            v2u vr[2][8];
            {
                const rsrc_t rv = RSRC(wuv, DC * 8192);
                const int vo = (128 * (cq >> 1) + 16 * (cq & 1) + 2 * kq) * 8192 + hpair * 512 + w * 128 + l15 * 8;
#pragma unroll
                for (int u = 0; u < 2; ++u)
#pragma unroll
                    for (int e = 0; e < 8; ++e) vr[u][e] = ld64_nt(rv, vo, (8 * u + (e >> 2) + 32 * (e & 3)) * 8192);
                SCHED();
            }
            BAR();
            STAMP(3); CYC0();
            const int qd = QOFF - w * 32768;
            constexpr int PD = 6;
            v4u kf[32], qf[32];
            v16f acc = {};
#pragma unroll
            for (int s = 0; s < PD; ++s) { kf[s] = *(LAS v4u*)(L + ad[s & 7] + (s >> 3) * 256); qf[s] = *(LAS v4u*)(L + (ad[s & 7] + qd) + (s >> 3) * 256); }
            SCHED();
#pragma unroll
            for (int s = 0; s < 32; ++s) {
                acc = mfma32(kf[s], qf[s], acc);
                if (s + PD < 32) { kf[s + PD] = *(LAS v4u*)(L + ad[(s + PD) & 7] + ((s + PD) >> 3) * 256); qf[s + PD] = *(LAS v4u*)(L + (ad[(s + PD) & 7] + qd) + ((s + PD) >> 3) * 256); }
                SCHED();
            }
            RAWBAR();
            STAMP(13); CYC1();
            float m = -3.0e38f;
#pragma unroll
            for (int i = 0; i < 16; ++i) m = fmaxf(m, acc[i]);
            m = __builtin_ceilf(max32(m, m));
            float p[16];
#pragma unroll
            for (int i = 0; i < 16; ++i) p[i] = __builtin_amdgcn_exp2f(acc[i] - m);
            const v4u f0 = {pkbf(p[0], p[1]), pkbf(p[2], p[3]), pkbf(p[4], p[5]), pkbf(p[6], p[7])};
            const v4u f1 = {pkbf(p[8], p[9]), pkbf(p[10], p[11]), pkbf(p[12], p[13]), pkbf(p[14], p[15])};
            *(LAS v4u*)(L + QOFF + (2 * w) * 1024 + lane * 16) = f0;
            *(LAS v4u*)(L + QOFF + (2 * w + 1) * 1024 + lane * 16) = f1;
            mx[w * 32 + (lane & 31)] = m;
            BAR();
            STAMP(4);
            float l = 0.f;
#pragma unroll
            for (int i = 0; i < 16; ++i) l += p[i];
            l = red32(l, l);
            ls[w * 32 + (lane & 31)] = l;
            BAR();
            if (w == 1 && lane < 16) {
                v2f mk[4], lk[4];
#pragma unroll
                for (int k = 0; k < 4; ++k) { mk[k] = *(LAS v2f*)(mx + 32 * k + 2 * lane); lk[k] = *(LAS v2f*)(ls + 32 * k + 2 * lane); }
                const float m0 = fmaxf(fmaxf(mk[0].x, mk[1].x), fmaxf(mk[2].x, mk[3].x)), m1 = fmaxf(fmaxf(mk[0].y, mk[1].y), fmaxf(mk[2].y, mk[3].y));
                float l0 = 0.f, l1 = 0.f;
#pragma unroll
                for (int k = 0; k < 4; ++k) { l0 += lk[k].x * __builtin_amdgcn_exp2f(mk[k].x - m0); l1 += lk[k].y * __builtin_amdgcn_exp2f(mk[k].y - m1); }
                st_sc1(pack4(m0, m1, l0, l1, tag), rws, WS_ST + a * 256 + lane * 16);
            }
            v4u vf[2][4];
            frag4(vr[0], vf[0]); frag4(vr[1], vf[1]);
            LAS u16* wt = (LAS u16*)(L + QOFF + 12288);
            if (w == 0) {
                v4u s0, s1;
                for (int sp = 0;; ++sp) {
                    MEMBAR();
                    s0 = ld_sc1(rws, WS_ST + lane * 256 + hpair * 16); s1 = ld_sc1(rws, WS_ST + (64 + lane) * 256 + hpair * 16);
                    if (__all(s0.w == tag && s1.w == tag) || sp > SPIN_MAX) break;
                    __builtin_amdgcn_s_sleep(1);
                }
                STAMP(6);
                const v4f sa = unpack4(s0), sb = unpack4(s1);
                float mm = max32(fmaxf(sa.x, sb.x), fmaxf(sa.y, sb.y));
                mm = row_max(max16(mm));
                auto sw = __builtin_amdgcn_permlane32_swap(f2u(mm), f2u(mm), false, false);
                const unsigned M0 = sw[0], M1 = sw[1];
                const float e00 = rb(__builtin_amdgcn_exp2f(sa.x - u2f(M0))), e01 = rb(__builtin_amdgcn_exp2f(sa.y - u2f(M1)));
                const float e10 = rb(__builtin_amdgcn_exp2f(sb.x - u2f(M0))), e11 = rb(__builtin_amdgcn_exp2f(sb.y - u2f(M1)));
                float ll = red32(e00 * sa.z + e10 * sb.z, e01 * sa.w + e11 * sb.w);
                ll = red16(ll, ll); ll = __builtin_amdgcn_rcpf(row_sum(ll));
                ((LAS float*)(L + QOFF + 12800))[half] = ll;
                wt[lane] = (u16)(f2u(e00) >> 16); wt[64 + lane] = (u16)(f2u(e10) >> 16); wt[128 + lane] = (u16)(f2u(e01) >> 16); wt[192 + lane] = (u16)(f2u(e11) >> 16);
            }
            const int io = WS_INB + a * 32768 + (32 * w + 8 * kq) * 256 + l15 * 16;
            BAR();
            v4u wa = *(LAS v4u*)(wt + (lane & 1) * 128 + 32 * w + 8 * kq);
            float rl = ((LAS float*)(L + QOFF + 12800))[w >> 1];
            STAMP(14);
            v4u d[8];
            for (int sp = 0;; ++sp) {
                MEMBAR();
#pragma unroll
                for (int e = 0; e < 8; ++e) d[e] = ld_sc1(rws, io + e * 256);
                const unsigned mxp = umax(umax(umax(d[0].x, d[1].x), umax(d[2].x, d[3].x)), umax(umax(d[4].x, d[5].x), umax(d[6].x, d[7].x)));
                if (__all(mxp != POISON) || sp > SPIN_MAX) break;
            }
            STAMP(7);
            float lat[2][4];
#pragma unroll
            for (int t = 0; t < 4; ++t) {
                v4u lo, hi;
#pragma unroll
                for (int p2 = 0; p2 < 4; ++p2) { const unsigned u0 = d[2 * p2][t], u1 = d[2 * p2 + 1][t]; lo[p2] = perm(u1, u0, 0x05040100u); hi[p2] = perm(u1, u0, 0x07060302u); }
                const v4f z = {0.f, 0.f, 0.f, 0.f};
                lat[0][t] = mfma16(wa, lo, z).x; lat[1][t] = mfma16(wa, hi, z).y;
            }
            LAS unsigned char* la = L + QOFF + 13312;
            {
                const v2u o0 = {pkbf(lat[0][0], lat[0][1]), pkbf(lat[0][2], lat[0][3])}, o1 = {pkbf(lat[1][0], lat[1][1]), pkbf(lat[1][2], lat[1][3])};
                if (lane < 16) { *(LAS v2u*)(la + w * 128 + l15 * 8) = o0; *(LAS v2u*)(la + 512 + w * 128 + l15 * 8) = o1; }
            }
            BAR();
            {
                LAS unsigned char* lr = la + (w >> 1) * 512 + (lane & 3) * 128 + kq * 16;
                const v4u a0 = *(LAS v4u*)lr, a1 = *(LAS v4u*)(lr + 64);
                v4f y[4] = {};
#pragma unroll
                for (int n = 0; n < 4; ++n) y[n] = mfma16(a0, vf[0][n], y[n]);
#pragma unroll
                for (int n = 0; n < 4; ++n) y[n] = mfma16(a1, vf[1][n], y[n]);
                const v2u o = {pkbf(sum4(y[0]) * rl, sum4(y[1]) * rl), pkbf(sum4(y[2]) * rl, sum4(y[3]) * rl)};
                if (lane < 16) st64_sc1(o, rws, WS_CTX + a * 512 + w * 128 + l15 * 8);
            }
            STAMP(8);
        } else {
            BAR();
            const int v = w - 4;
            const int col = lane & 31;
            int ob = WS_INB + ((2 * half) * 8 + 2 * v + (col >> 4)) * 32768 + a * 256 + (col & 15) * 16;
            PIN(ob);
            RAWBAR();
            v4s vb[4][16];
#define VREAD(t) _Pragma("unroll") for (int k = 0; k < 16; ++k) vb[t][k] = __builtin_amdgcn_ds_read_tr16_b64_v4i16((LAS v4s*)(L + (ad[2 * (t) + (k & 1)] + (k >> 3) * 65536) + ((k >> 1) & 3) * 16384))
            VREAD(0); VREAD(1);
            BAR();
            STAMP(4); CYC0();
            v4u pa[8];
            const float m0 = mx[col], m1 = mx[32 + col], m2 = mx[64 + col], m3 = mx[96 + col];
#pragma unroll
            for (int ks = 0; ks < 8; ++ks) pa[ks] = *(LAS v4u*)(L + QOFF + ks * 1024 + lane * 16);
            SCHED();
            const float mm = fmaxf(fmaxf(fmaxf(m0, m1), m2), m3);
            const float mk[4] = {m0, m1, m2, m3};
            unsigned pk[8][4];
            v16f acc[4];
#pragma unroll
            for (int t = 0; t < 4; ++t) {
#pragma unroll
                for (int ks = 0; ks < 8; ++ks) {
                    if (t == 0) {
                        const unsigned sub = (unsigned)fminf(mm - mk[ks >> 1], 255.f) * 0x00800080u;
#pragma unroll
                        for (int i = 0; i < 4; ++i) { const unsigned u = pa[ks][i]; pa[ks][i] = __builtin_bit_cast(unsigned, __builtin_elementwise_sub_sat(__builtin_bit_cast(v2us, u), __builtin_bit_cast(v2us, sub))); }
                    }
                    const v4u bv = __builtin_bit_cast(v4u, __builtin_shufflevector(vb[t][2 * ks], vb[t][2 * ks + 1], 0, 1, 2, 3, 4, 5, 6, 7));
                    const v16f z = {};
                    acc[t] = mfma32(pa[ks], bv, ks ? acc[t] : z);
                    if (t < 2) {
                        vb[t + 2][2 * ks] = __builtin_amdgcn_ds_read_tr16_b64_v4i16((LAS v4s*)(L + (ad[2 * (t + 2)] + (ks >> 2) * 65536) + (ks & 3) * 16384));
                        vb[t + 2][2 * ks + 1] = __builtin_amdgcn_ds_read_tr16_b64_v4i16((LAS v4s*)(L + (ad[2 * (t + 2) + 1] + (ks >> 2) * 65536) + (ks & 3) * 16384));
                    }
                    if (t > 0 && ks >= 2 && ks < 6) {
                        pk[2 * (ks - 2)][t - 1] = pkbf(acc[t - 1][4 * (ks - 2)], acc[t - 1][4 * (ks - 2) + 1]);
                        pk[2 * (ks - 2) + 1][t - 1] = pkbf(acc[t - 1][4 * (ks - 2) + 2], acc[t - 1][4 * (ks - 2) + 3]);
                    }
                    if (t == 0 && ks == 5) RAWBAR();
                    SCHED();
                }
            }
            STAMP(13); CYC1();
#pragma unroll
            for (int e = 0; e < 8; ++e) {
                const v4u o = {pk[e][0], pk[e][1], pk[e][2], pkbf(acc[3][2 * e], acc[3][2 * e + 1])};
                __builtin_amdgcn_raw_buffer_store_b128(o, rws, ob, (4 * (e >> 1) + (e & 1)) * 8 * 32768, AUX_SC1);
            }
            STAMP(5);
        }
    }
    STAMP_FLUSH();
}

extern "C" void kernel_launch(void* const* d_in, const int* in_sizes, int n_in, void* d_out, int out_size, void* d_ws, size_t ws_size, hipStream_t stream) {
    fused_kernel<<<256, NT, 0, stream>>>((const u16*)d_in[0], (const u16*)d_in[1], (const u16*)d_in[2], (const u16*)d_in[3], (const u16*)d_in[4], (const u16*)d_in[5],
                                         (const u16*)d_in[7], (const u16*)d_in[8], (const u16*)d_in[9], (const u16*)d_in[10], (const u16*)d_in[11], (u16*)d_out, (unsigned char*)d_ws);
}
```

```cpp
#include <hip/hip_runtime.h>
#include <hip/hip_bf16.h>

#define STAMPS 1
#define PROBE 1
#define LATE_DELAY 400
#define A_DELAY 0
#define QWAIT 2

typedef unsigned short u16;
typedef unsigned long long u64;
typedef unsigned v4u __attribute__((ext_vector_type(4)));
typedef unsigned v2u __attribute__((ext_vector_type(2)));
typedef float v4f __attribute__((ext_vector_type(4)));
typedef float v2f __attribute__((ext_vector_type(2)));
typedef float v16f __attribute__((ext_vector_type(16)));
typedef short v4s __attribute__((ext_vector_type(4)));
typedef unsigned short v2us __attribute__((ext_vector_type(2)));
typedef __bf16 v2bf __attribute__((ext_vector_type(2)));
typedef __bf16 v8bf __attribute__((ext_vector_type(8)));
#define LAS __attribute__((address_space(3)))
#define GAS __attribute__((address_space(1)))
typedef __amdgpu_buffer_rsrc_t rsrc_t;

constexpr int M = 576, DC = 512, SK = 16384, FF = 4608;
constexpr int NT = 512;
constexpr int CODE_BYTES = 24320;
constexpr int SPIN_MAX = 1 << 16;
constexpr unsigned POISON = 0xFFFFFFFFu;
constexpr int OOB = 0x40000000;
constexpr int WS_EPOCH = 0, WS_F1 = 256;
constexpr int WS_CL = 4096;
constexpr int WS_H1 = WS_CL + 128 * 128;
constexpr int WS_HQ = WS_H1 + 4 * 288 * 16;
constexpr int WS_ST = WS_HQ + 32 * 4 * 512;
constexpr int WS_QA = 256 * 1024;
constexpr int WS_PZ = WS_QA + 32768;
constexpr int WS_CTX = WS_PZ;
constexpr int WS_U5 = WS_CTX + 65536;
constexpr int WS_T5 = WS_U5 + 4608;
constexpr int WS_MRB = WS_T5 + 9216;
constexpr int WS_H6 = WS_MRB + 4608;
constexpr int WS_INB = 1 << 20;
constexpr int WS_STAMP = WS_INB + (4 << 20);
constexpr int WS_BYTES = WS_STAMP + 256 * 8 * 16 * 4;
static_assert(WS_ST + 32768 <= WS_QA && WS_H6 + 72 * 2304 <= WS_PZ + 8 * 32768 && WS_PZ + 8 * 32768 <= WS_INB, "ws map");
static_assert(WS_H1 % 128 == 0 && WS_HQ % 128 == 0 && WS_ST % 128 == 0 && WS_U5 % 128 == 0 && WS_T5 % 128 == 0 && WS_MRB % 128 == 0 && WS_H6 % 128 == 0, "line aligned");

#define RSRC(p, bytes) __builtin_amdgcn_make_buffer_rsrc((void*)(p), 0, (bytes), 0x00020000)
#define AUX_NT 2
#define AUX_SC1 16
__device__ __forceinline__ v4u ld_nt(rsrc_t r, int off) { return __builtin_amdgcn_raw_buffer_load_b128(r, off, 0, AUX_NT); }
__device__ __forceinline__ v2u ld64_nt(rsrc_t r, int off, int so) { return __builtin_amdgcn_raw_buffer_load_b64(r, off, so, AUX_NT); }
__device__ __forceinline__ v4u ld_sc1(rsrc_t r, int off) { return __builtin_amdgcn_raw_buffer_load_b128(r, off, 0, AUX_SC1); }
__device__ __forceinline__ unsigned ld32_sc1(rsrc_t r, int off) { return __builtin_amdgcn_raw_buffer_load_b32(r, off, 0, AUX_SC1); }
__device__ __forceinline__ void st_sc1(v4u v, rsrc_t r, int off) { __builtin_amdgcn_raw_buffer_store_b128(v, r, off, 0, AUX_SC1); }
__device__ __forceinline__ void st64_sc1(v2u v, rsrc_t r, int off) { __builtin_amdgcn_raw_buffer_store_b64(v, r, off, 0, AUX_SC1); }
__device__ __forceinline__ void st32_sc1(unsigned v, rsrc_t r, int off) { __builtin_amdgcn_raw_buffer_store_b32(v, r, off, 0, AUX_SC1); }
#define MEMBAR() asm volatile("" ::: "memory")
#define DRAIN() asm volatile("s_waitcnt vmcnt(0)" ::: "memory")
#define SCHED() __builtin_amdgcn_sched_barrier(0)
#define PIN(x) asm volatile("" : "+v"(x))
#define BAR() do { asm volatile("s_waitcnt lgkmcnt(0)\n\ts_barrier" ::: "memory"); } while (0)
#define RAWBAR() do { asm volatile("s_barrier" ::: "memory"); } while (0)

__device__ __forceinline__ float u2f(unsigned u) { return __builtin_bit_cast(float, u); }
__device__ __forceinline__ unsigned f2u(float f) { return __builtin_bit_cast(unsigned, f); }
__device__ __forceinline__ float bflo(unsigned u) { return u2f(u << 16); }
__device__ __forceinline__ float bfhi(unsigned u) { return u2f(u & 0xFFFF0000u); }
__device__ __forceinline__ unsigned pkbf(float lo, float hi) { v2f p = {lo, hi}; v2bf q = __builtin_convertvector(p, v2bf); return __builtin_bit_cast(unsigned, q); }
__device__ __forceinline__ float rb(float f) { return bflo(pkbf(f, f)); }
__device__ __forceinline__ float dot2(unsigned a, unsigned b, float c) { return __builtin_amdgcn_fdot2_f32_bf16(__builtin_bit_cast(v2bf, a), __builtin_bit_cast(v2bf, b), c, false); }
__device__ __forceinline__ unsigned perm(unsigned hi, unsigned lo, unsigned sel) { return __builtin_amdgcn_perm(hi, lo, sel); }
__device__ __forceinline__ unsigned umax(unsigned a, unsigned b) { return a > b ? a : b; }
__device__ __forceinline__ v4f mfma16(v4u a, v4u b, v4f c) { return __builtin_amdgcn_mfma_f32_16x16x32_bf16(__builtin_bit_cast(v8bf, a), __builtin_bit_cast(v8bf, b), c, 0, 0, 0); }
__device__ __forceinline__ v16f mfma32(v4u a, v4u b, v16f c) { return __builtin_amdgcn_mfma_f32_32x32x16_bf16(__builtin_bit_cast(v8bf, a), __builtin_bit_cast(v8bf, b), c, 0, 0, 0); }
__device__ __forceinline__ float sum4(v4f a) { return (a.x + a.z) + (a.y + a.w); }
__device__ __forceinline__ v4u pack4(float a, float b, float c, float d, unsigned tag) {
    const unsigned ud = f2u(d);
    v4u g; g.x = perm(f2u(a), ud, 0x07060501u); g.y = perm(f2u(b), ud, 0x07060502u); g.z = perm(f2u(c), ud, 0x07060503u); g.w = tag; return g;
}
__device__ __forceinline__ v4f unpack4(v4u g) {
    const unsigned gx = g.x, gy = g.y, gz = g.z;
    const unsigned t = perm(gy, gx, 0x0c04000cu);
    const unsigned d = perm(gz, t, 0x04020100u);
    v4f r = {u2f(gx & 0xFFFFFF00u), u2f(gy & 0xFFFFFF00u), u2f(gz & 0xFFFFFF00u), u2f(d)}; return r;
}
__device__ __forceinline__ float red32(float a, float b) { auto r = __builtin_amdgcn_permlane32_swap(f2u(a), f2u(b), false, false); const unsigned x = r[0], y = r[1]; return u2f(x) + u2f(y); }
__device__ __forceinline__ float red16(float a, float b) { auto r = __builtin_amdgcn_permlane16_swap(f2u(a), f2u(b), false, false); const unsigned x = r[0], y = r[1]; return u2f(x) + u2f(y); }
__device__ __forceinline__ float max32(float a, float b) { auto r = __builtin_amdgcn_permlane32_swap(f2u(a), f2u(b), false, false); const unsigned x = r[0], y = r[1]; return fmaxf(u2f(x), u2f(y)); }
__device__ __forceinline__ float max16(float a) { auto r = __builtin_amdgcn_permlane16_swap(f2u(a), f2u(a), false, false); const unsigned x = r[0], y = r[1]; return fmaxf(u2f(x), u2f(y)); }
template <int CTRL> __device__ __forceinline__ float dpp(float v) { return u2f((unsigned)__builtin_amdgcn_update_dpp(0, (int)f2u(v), CTRL, 0xf, 0xf, true)); }
__device__ __forceinline__ float add_x8(float v) { return v + dpp<0x128>(v); }
__device__ __forceinline__ float add_r4(float v) { return v + dpp<0x124>(v); }
__device__ __forceinline__ float add_x2(float v) { return v + dpp<0x4E>(v); }
__device__ __forceinline__ float add_x1(float v) { return v + dpp<0xB1>(v); }
__device__ __forceinline__ float row_sum(float v) { return add_r4(add_x8(add_x2(add_x1(v)))); }
__device__ __forceinline__ float row_max(float v) { v = fmaxf(v, dpp<0xB1>(v)); v = fmaxf(v, dpp<0x4E>(v)); v = fmaxf(v, dpp<0x128>(v)); return fmaxf(v, dpp<0x124>(v)); }
__device__ __forceinline__ float wave_sum(float v) { v = row_sum(v); v = red16(v, v); return red32(v, v); }
__device__ __forceinline__ void interleave(v4u& a, v4u& b) {
    const unsigned a0 = a.x, a1 = a.y, a2 = a.z, a3 = a.w, b0 = b.x, b1 = b.y, b2 = b.z, b3 = b.w;
    a.x = perm(b0, a0, 0x05040100u); a.y = perm(b0, a0, 0x07060302u); a.z = perm(b1, a1, 0x05040100u); a.w = perm(b1, a1, 0x07060302u);
    b.x = perm(b2, a2, 0x05040100u); b.y = perm(b2, a2, 0x07060302u); b.z = perm(b3, a3, 0x05040100u); b.w = perm(b3, a3, 0x07060302u);
}
__device__ __forceinline__ void frag4(const v2u* r, v4u* f) {
    unsigned o[4][4];
#pragma unroll
    for (int p = 0; p < 4; ++p) {
        const unsigned ax = r[2 * p].x, ay = r[2 * p].y, bx = r[2 * p + 1].x, by = r[2 * p + 1].y;
        o[0][p] = perm(bx, ax, 0x05040100u); o[1][p] = perm(bx, ax, 0x07060302u); o[2][p] = perm(by, ay, 0x05040100u); o[3][p] = perm(by, ay, 0x07060302u);
    }
#pragma unroll
    for (int n = 0; n < 4; ++n) { f[n] = v4u{o[n][0], o[n][1], o[n][2], o[n][3]}; PIN(f[n]); }
}
__device__ __forceinline__ void dot8(float (&acc)[8], const v4u& a, const v4u& b, unsigned xp) {
    const unsigned a0 = a.x, a1 = a.y, a2 = a.z, a3 = a.w, b0 = b.x, b1 = b.y, b2 = b.z, b3 = b.w;
    acc[0] = dot2(a0, xp, acc[0]); acc[1] = dot2(a1, xp, acc[1]); acc[2] = dot2(a2, xp, acc[2]); acc[3] = dot2(a3, xp, acc[3]);
    acc[4] = dot2(b0, xp, acc[4]); acc[5] = dot2(b1, xp, acc[5]); acc[6] = dot2(b2, xp, acc[6]); acc[7] = dot2(b3, xp, acc[7]);
}
__device__ __forceinline__ void dot8z(float (&acc)[8], const v4u& a, unsigned slo, unsigned shi) {
    const unsigned a0 = a.x, a1 = a.y, a2 = a.z, a3 = a.w;
    acc[0] = dot2(a0, slo, acc[0]); acc[1] = dot2(a0, shi, acc[1]); acc[2] = dot2(a1, slo, acc[2]); acc[3] = dot2(a1, shi, acc[3]);
    acc[4] = dot2(a2, slo, acc[4]); acc[5] = dot2(a2, shi, acc[5]); acc[6] = dot2(a3, slo, acc[6]); acc[7] = dot2(a3, shi, acc[7]);
}
#define ZERO8(acc) float acc[8] = {0.f, 0.f, 0.f, 0.f, 0.f, 0.f, 0.f, 0.f}; asm volatile("" : "+v"(acc[0]), "+v"(acc[1]), "+v"(acc[2]), "+v"(acc[3]), "+v"(acc[4]), "+v"(acc[5]), "+v"(acc[6]), "+v"(acc[7]))
__device__ __forceinline__ float red8cols(const float (&a)[8], int lane) {
    const float b0 = red32(a[0], a[4]), b1 = red32(a[1], a[5]), b2 = red32(a[2], a[6]), b3 = red32(a[3], a[7]);
    const float c0 = red16(b0, b2), c1 = red16(b1, b3);
    const float d0 = add_x8(c0), d1 = add_x8(c1);
    return (lane & 8) ? d1 : d0;
}
__device__ __forceinline__ float silu_bf(float v) { return rb(v * __builtin_amdgcn_rcpf(1.f + __expf(-v))); }
__device__ __forceinline__ u64 rt() { return __builtin_amdgcn_s_memrealtime(); }
#define STAMP(i) do { SCHED(); ts[i] = (unsigned)rt(); SCHED(); } while (0)
__device__ __forceinline__ int stamp_group_d(int b) { return b >= 128 ? 0 : (b >= 56 ? 1 : (b >= 20 ? 2 : (b < 18 ? 3 : 4))); }
#define STAMP_FLUSH() do { if (stamp_group_d(blockIdx.x) == 3 && w == 0) { const unsigned lim = 4000u + (ts[0] ? 4u * (ts[0] - ts[0]) : 0u); while ((unsigned)rt() - ts[0] < lim) __builtin_amdgcn_s_sleep(2); } } while (0)
#define CYC0() SCHED(); const unsigned cyc0_ = (unsigned)__builtin_amdgcn_s_memtime(); SCHED()
#define CYC1() do { SCHED(); ts[15] = (unsigned)__builtin_amdgcn_s_memtime() - cyc0_; SCHED(); } while (0)

__global__ void __launch_bounds__(NT, 2) fused_kernel(const u16* __restrict__ x, const u16* __restrict__ ckv, const u16* __restrict__ wq, const u16* __restrict__ wuk,
                                                      const u16* __restrict__ wuv, const u16* __restrict__ wo, const u16* __restrict__ wg, const u16* __restrict__ wu,
                                                      const u16* __restrict__ wd, const u16* __restrict__ cc, const u16* __restrict__ wmod, u16* __restrict__ out,
                                                      unsigned char* __restrict__ ws) {
    __shared__ __attribute__((aligned(16))) unsigned char smem[163840];
    LAS unsigned char* L = (LAS unsigned char*)smem;
    u64 pc0; asm volatile("s_getpc_b64 %0" : "=s"(pc0));
    const u64 t_start = rt();
    asm volatile("" : "+s"(x), "+s"(ckv), "+s"(wq), "+s"(wuk), "+s"(wuv), "+s"(wo), "+s"(wg), "+s"(wu), "+s"(wd), "+s"(cc), "+s"(wmod), "+s"(out), "+s"(ws));
    const int bid = blockIdx.x, tid = threadIdx.x, lane = tid & 63, w = __builtin_amdgcn_readfirstlane(tid >> 6);
    const rsrc_t rws = RSRC(ws, WS_BYTES);
    unsigned ts[16] = {0, 0, 0, 0, 0, 0, 0, 0, 0, 0, 0, 0, 0, 0, 0, 0};
    ts[0] = (unsigned)t_start;
    const rsrc_t rx = RSRC(x, M * 2);
    const int l15 = lane & 15, kq = lane >> 4, half = lane >> 5;

    if (__builtin_expect(bid < 128, 1)) {
        const int b = bid, hh = b >> 2, mj = b & 3;
        const rsrc_t rmod = RSRC(wmod, M * 6 * M * 2), rc = RSRC(cc, M * 2);
        LAS float* red = (LAS float*)L;
        LAS unsigned* hp = (LAS unsigned*)(L + 20480);
        const bool doS1 = b < 72;
        unsigned tag;
        if (__builtin_expect(w < 4, 1)) {
            const int kp = b / 18, nt = b - 18 * kp;
            const int r1 = 144 * kp + 36 * w + (lane >> 3);
            const int co = doS1 ? r1 * 2 : OOB, wo1 = doS1 ? r1 * 6912 + nt * 128 + (lane & 7) * 16 : OOB;
            unsigned s1c[5]; v4u s1w[5], q2[9];
#pragma unroll
            for (int i = 0; i < 5; ++i) s1c[i] = __builtin_amdgcn_raw_buffer_load_b16(rc, (i < 4 || lane < 32) ? co + 16 * i : OOB, 0, 0);
#pragma unroll
            for (int i = 0; i < 5; ++i) s1w[i] = ld_nt(rmod, (i < 4 || lane < 32) ? wo1 + 8 * i * 6912 : OOB);
            tag = ld32_sc1(rws, WS_EPOCH);
            const rsrc_t rq = RSRC(wq, M * 8192);
            const int qo = (144 * mj + 36 * w) * 8192 + hh * 256 + l15 * 16;
#pragma unroll
            for (int i = 0; i < 8; ++i) q2[i] = ld_nt(rq, qo + (8 * (i >> 1) + 2 * kq + (i & 1)) * 8192);
            q2[8] = ld_nt(rq, qo + (32 + kq) * 8192);
            SCHED();
            STAMP(9);
            if (doS1) {
                unsigned slo[5], shi[5];
#pragma unroll
                for (int i = 0; i < 5; ++i) { shi[i] = f2u(silu_bf(bflo(s1c[i]))); slo[i] = shi[i] >> 16; PIN(shi[i]); PIN(slo[i]); }
                ZERO8(acc);
                SCHED();
#pragma unroll
                for (int i = 0; i < 5; ++i) dot8z(acc, s1w[i], slo[i], shi[i]);
                red[w * 64 + (lane & 7) * 8 + (lane >> 3)] = red8cols(acc, lane);
                STAMP(10);
                BAR();
                if (tid < 16) {
                    const v4f s = (*(LAS v4f*)(red + tid * 4) + *(LAS v4f*)(red + 64 + tid * 4)) + (*(LAS v4f*)(red + 128 + tid * 4) + *(LAS v4f*)(red + 192 + tid * 4));
                    st_sc1(pack4(s.x, s.y, s.z, s.w, tag + 1u), rws, WS_H1 + kp * 4608 + (nt * 16 + tid) * 16);
                }
                STAMP(1);
                BAR();
            }
            PIN(tag); tag += 1u;
#pragma unroll
            for (int i = 0; i < 4; ++i) { interleave(q2[2 * i], q2[2 * i + 1]); PIN(q2[2 * i]); PIN(q2[2 * i + 1]); }
            PIN(q2[8]);
            ZERO8(acc);
            STAMP(12);
            BAR();
            STAMP(2);
            {
                const unsigned h0 = hp[18 * w + kq], h1 = hp[18 * w + 4 + kq], h2 = hp[18 * w + 8 + kq], h3 = hp[18 * w + 12 + kq];
                const unsigned hs = ((LAS u16*)hp)[36 * w + 32 + kq];
                dot8(acc, q2[0], q2[1], h0); dot8(acc, q2[2], q2[3], h1); dot8(acc, q2[4], q2[5], h2); dot8(acc, q2[6], q2[7], h3);
                dot8z(acc, q2[8], hs, hs << 16);
                const float b0 = red32(acc[0], acc[4]), b1 = red32(acc[1], acc[5]), b2 = red32(acc[2], acc[6]), b3 = red32(acc[3], acc[7]);
                v2f c; c.x = red16(b0, b2); c.y = red16(b1, b3);
                *(LAS v2f*)(red + w * 128 + l15 * 8 + kq * 2) = c;
            }
            BAR();
            if (tid < 32) {
                const v4f s = (*(LAS v4f*)(red + tid * 4) + *(LAS v4f*)(red + 128 + tid * 4)) + (*(LAS v4f*)(red + 256 + tid * 4) + *(LAS v4f*)(red + 384 + tid * 4));
                st_sc1(pack4(s.x, s.y, s.z, s.w, tag), rws, WS_HQ + b * 512 + tid * 16);
            }
            STAMP(3);
            if (w == 1) {
                const int of = lane == 0 ? WS_F1 : (lane < 9 ? WS_CL + lane * 128 : WS_CL + b * 128);
                for (int sp = 0;; ++sp) {
                    MEMBAR();
                    const unsigned f = ld32_sc1(rws, of);
                    if (__all(f == tag) || sp > SPIN_MAX) break;
                }
            }
            BAR();
            if (tid < 16) st_sc1(*(LAS v4u*)(L + 24576 + tid * 16), rws, WS_QA + hh * 1024 + mj * 256 + tid * 16);
            STAMP(5);
        } else {
            tag = ld32_sc1(rws, WS_EPOCH);
            float npair0 = 0.f, npair1 = 0.f;
            const int j = (lane & 31) < 18 ? (lane & 31) : 17;
            if (w < 6) {
                const v4u xa = __builtin_amdgcn_raw_buffer_load_b128(rx, lane * 16, 0, 0);
                const unsigned xb = __builtin_amdgcn_raw_buffer_load_b32(rx, 1024 + lane * 4, 0, 0);
                const int m0 = 144 * mj + 4 * (18 * (w - 4) + j) + 2 * half;
                const unsigned xm = __builtin_amdgcn_raw_buffer_load_b32(rx, m0 * 2, 0, 0);
                if (doS1) { BAR(); BAR(); }
                const unsigned a0 = xa.x, a1 = xa.y, a2 = xa.z, a3 = xa.w;
                float v[10] = {bflo(a0), bfhi(a0), bflo(a1), bfhi(a1), bflo(a2), bfhi(a2), bflo(a3), bfhi(a3), bflo(xb), bfhi(xb)};
                float s = 0.f;
#pragma unroll
                for (int i = 0; i < 10; ++i) s += v[i];
                const float mean = wave_sum(s) * (1.f / M);
                float q = 0.f;
#pragma unroll
                for (int i = 0; i < 8; ++i) q += (v[i] - mean) * (v[i] - mean);
                if (lane < 32) q += (v[8] - mean) * (v[8] - mean) + (v[9] - mean) * (v[9] - mean);
                const float var = rb(wave_sum(q) * (1.f / M));
                const float rden = __builtin_amdgcn_rcpf(rb(__builtin_amdgcn_sqrtf(rb(var + 1e-5f)))), mb = rb(mean);
                npair0 = rb(rb(bflo(xm) - mb) * rden); npair1 = rb(rb(bfhi(xm) - mb) * rden);
            } else if (doS1) { BAR(); BAR(); }
            v4u k3[8];
            {
                const rsrc_t rk = RSRC(wuk, DC * 8192);
                const int ko = (128 * mj + 32 * (w - 4) + kq) * 8192 + hh * 256 + l15 * 16;
#pragma unroll
                for (int i = 0; i < 8; ++i) k3[i] = ld_nt(rk, ko + 4 * i * 8192);
                SCHED();
            }
            PIN(tag); tag += 1u;
            PIN(npair0); PIN(npair1);
            STAMP(11);
            if (w < 6) {
                const int cg = 36 * mj + 18 * (w - 4) + j;
                const int o = WS_H1 + (2 * half) * 4608 + cg * 16;
                v4u g0, g1, g2, g3;
                for (int sp = 0;; ++sp) {
                    MEMBAR();
                    g0 = ld_sc1(rws, o); g1 = ld_sc1(rws, o + 4608); g2 = ld_sc1(rws, o + 2304); g3 = ld_sc1(rws, o + 2304 + 4608);
                    const bool ok = g0.w == tag && g1.w == tag && g2.w == tag && g3.w == tag;
                    if (__all(ok) || sp > SPIN_MAX) break;
                }
                STAMP(12);
                const v4f sa = unpack4(g0) + unpack4(g1), ca = unpack4(g2) + unpack4(g3);
                const unsigned sp = pkbf(red32(sa.x, sa.z), red32(sa.y, sa.w)), cp = pkbf(red32(ca.x, ca.z), red32(ca.y, ca.w));
                const unsigned c1 = pkbf(1.f + bflo(cp), 1.f + bfhi(cp));
                const unsigned t1 = pkbf(npair0 * bflo(c1), npair1 * bfhi(c1));
                hp[2 * (18 * (w - 4) + j) + half] = pkbf(bflo(t1) + bflo(sp), bfhi(t1) + bfhi(sp));
            }
            BAR();
            BAR();
            {
                const int o = WS_HQ + (hh * 4 + kq) * 512 + l15 * 32;
                v4u g0, g1;
                for (int sp = 0;; ++sp) {
                    MEMBAR();
                    g0 = ld_sc1(rws, o); g1 = ld_sc1(rws, o + 16);
                    if (__all(g0.w == tag && g1.w == tag) || sp > SPIN_MAX) break;
                }
                STAMP(4);
                const v4f qa = unpack4(g0), qb = unpack4(g1);
                const float b0 = red32(qa.x, qb.x), b1 = red32(qa.y, qb.y), b2 = red32(qa.z, qb.z), b3 = red32(qa.w, qb.w);
                const unsigned pp = pkbf(red16(b0, b2), red16(b1, b3));
                auto s16 = __builtin_amdgcn_permlane16_swap(pp, pp, false, false);
                const unsigned pe = s16[0], po = s16[1];
                auto se = __builtin_amdgcn_permlane32_swap(pe, pe, false, false);
                auto so = __builtin_amdgcn_permlane32_swap(po, po, false, false);
                const unsigned p0 = se[0], p2 = se[1], p1 = so[0], p3 = so[1];
                float a[8];
#pragma unroll
                for (int i = 0; i < 8; ++i) { const unsigned k0 = k3[i].x, k1 = k3[i].y, k2 = k3[i].z, k3w = k3[i].w; a[i] = dot2(k3w, p3, dot2(k2, p2, dot2(k1, p1, dot2(k0, p0, 0.f)))); }
                float c[4];
#pragma unroll
                for (int i = 0; i < 4; ++i) { const float u0 = add_x8(a[i]), u1 = add_x8(a[i + 4]); c[i] = (lane & 8) ? u1 : u0; }
                const float e0 = add_x2(c[0]), e1 = add_x2(c[1]), e2 = add_x2(c[2]), e3 = add_x2(c[3]);
                const float f0 = (lane & 2) ? e2 : e0, f1 = (lane & 2) ? e3 : e1;
                const float g0f = add_x1(f0), g1f = add_x1(f1);
                float r = (lane & 1) ? g1f : g0f;
                r += dpp<0x141>(dpp<0x1B>(r));
                r *= 0.08838834764831845f * 1.4426950408889634f;
                LAS u16* qo = (LAS u16*)(L + 24576);
                const int i = ((lane >> 1) & 4) | (lane & 3);
                qo[32 * (w - 4) + 4 * i + kq] = (u16)pkbf(r, r);
            }
            BAR();
            STAMP(5);
        }
        if (LATE_DELAY) { while (rt() - t_start < (u64)LATE_DELAY) __builtin_amdgcn_s_sleep(1); }
        if (b >= 56) {
            const int f = b - 56;
            const int ks0 = 9 * (w & 1) + (w < 4 ? 0 : 5);
            v4u gf[5][4], df[2][4], ef[2][4];
            {
                const rsrc_t rg = RSRC((w & 2) ? wu : wg, M * FF * 2), rd = RSRC(wd, M * FF * 2);
                const int go = (32 * ks0 + 8 * kq) * 9216 + f * 128 + l15 * 8;
                const int dofs = (64 * f + 8 * kq) * 1152 + l15 * 8;
                v2u gr[5][8], dr[2][8], er[2][8];
#pragma unroll
                for (int u = 0; u < 4; ++u)
#pragma unroll
                    for (int e = 0; e < 8; ++e) gr[u][e] = ld64_nt(rg, go, (32 * u + e) * 9216);
#pragma unroll
                for (int u = 0; u < 2; ++u)
#pragma unroll
                    for (int e = 0; e < 8; ++e) dr[u][e] = ld64_nt(rd, dofs, (32 * u + e) * 1152 + w * 128);
                if (w < 4) {
#pragma unroll
                    for (int e = 0; e < 8; ++e) gr[4][e] = ld64_nt(rg, go, (128 + e) * 9216);
                    SCHED();
                    frag4(gr[4], gf[4]);
                } else if (w == 5) {
#pragma unroll
                    for (int u = 0; u < 2; ++u)
#pragma unroll
                        for (int e = 0; e < 8; ++e) er[u][e] = ld64_nt(rd, dofs, (32 * u + e) * 1152 + 1024);
                    SCHED();
                    frag4(er[0], ef[0]); frag4(er[1], ef[1]);
                }
                SCHED();
#pragma unroll
                for (int u = 0; u < 4; ++u) frag4(gr[u], gf[u]);
                frag4(dr[0], df[0]); frag4(dr[1], df[1]);
            }
            STAMP(6);
            LAS float* P = (LAS float*)L;
            LAS float* Sx = (LAS float*)(L + 8192);
            float a1g, a1u, a2g, a2u;
            {
                const int ao = (l15 == 0 ? WS_MRB + 2 * 1152 : (l15 == 4 ? WS_MRB + 1152 : OOB)) + (32 * ks0 + 8 * kq) * 2;
                v4u a[5];
                for (int sp = 0;; ++sp) {
                    MEMBAR();
#pragma unroll
                    for (int u = 0; u < 5; ++u) a[u] = ld_sc1(rws, (u < 4 || w < 4) ? ao + 64 * u : OOB);
                    unsigned mx = 0;
#pragma unroll
                    for (int u = 0; u < 5; ++u) mx = umax(umax(mx, a[u].x), a[u].z);
                    if (__all(mx != POISON) || sp > SPIN_MAX) break;
                    __builtin_amdgcn_s_sleep(2);
                }
                v4f acc[4] = {};
#pragma unroll
                for (int u = 0; u < 4; ++u)
#pragma unroll
                    for (int n = 0; n < 4; ++n) acc[n] = mfma16(a[u], gf[u][n], acc[n]);
                if (w < 4) {
#pragma unroll
                    for (int n = 0; n < 4; ++n) acc[n] = mfma16(a[4], gf[4][n], acc[n]);
                }
                const v4f o = {acc[0].x, acc[1].x, acc[2].x, acc[3].x};
                if (lane < 32) *(LAS v4f*)(P + 512 + kq * 512 + w * 64 + l15 * 4) = o;
                BAR();
                LAS float* Q1 = P + 512 + lane; LAS float* Q2 = P + 1024 + lane;
                a1g = (Q1[0] + Q1[64]) + (Q1[256] + Q1[320]); a1u = (Q1[128] + Q1[192]) + (Q1[384] + Q1[448]);
                a2g = (Q2[0] + Q2[64]) + (Q2[256] + Q2[320]); a2u = (Q2[128] + Q2[192]) + (Q2[384] + Q2[448]);
                PIN(a1g); PIN(a1u); PIN(a2g); PIN(a2u);
            }
            STAMP(13);
            const int uo = (l15 < 4 ? WS_U5 + l15 * 1152 : OOB) + (32 * ks0 + 8 * kq) * 2;
            v4f acc[4] = {};
            float rs = 0.f, kg = 0.f, ku = 0.f;
            if (w < 4) {
                v4u a[5];
                for (int sp = 0;; ++sp) {
                    MEMBAR();
#pragma unroll
                    for (int u = 0; u < 5; ++u) a[u] = ld_sc1(rws, uo + 64 * u);
                    unsigned mx = 0;
#pragma unroll
                    for (int u = 0; u < 5; ++u) mx = umax(umax(umax(umax(mx, a[u].x), a[u].y), a[u].z), a[u].w);
                    if (__all(mx != POISON) || sp > SPIN_MAX) break;
                }
                STAMP(7);
#pragma unroll
                for (int u = 0; u < 5; ++u)
#pragma unroll
                    for (int n = 0; n < 4; ++n) acc[n] = mfma16(a[u], gf[u][n], acc[n]);
                SCHED();
                const v4f o = {sum4(acc[0]), sum4(acc[1]), sum4(acc[2]), sum4(acc[3])};
                if (lane < 16) *(LAS v4f*)(P + w * 64 + l15 * 4) = o;
                RAWBAR();
                if (w == 0) {
                    const float mean = sum4(*(LAS v4f*)Sx) * (1.f / M);
                    const float var = rb(fmaxf(sum4(*(LAS v4f*)(Sx + 4)) * (1.f / M) - mean * mean, 0.f));
                    rs = __builtin_amdgcn_rcpf(rb(__builtin_amdgcn_sqrtf(rb(var + 1e-5f))));
                    const float mr = rb(mean) * rs;
                    kg = a2g - mr * a1g; ku = a2u - mr * a1u;
                    PIN(rs); PIN(kg); PIN(ku);
                }
            } else {
                v4u a[4], t[3];
                const int to = WS_T5 + kq * 2304 + ((w - 4) * 2 * 16 + l15) * 16;
                const int to2 = w == 7 ? WS_T5 + kq * 2304 + (128 + l15) * 16 : OOB;
                for (int sp = 0;; ++sp) {
                    MEMBAR();
                    t[0] = ld_sc1(rws, to); t[1] = ld_sc1(rws, to + 256); t[2] = ld_sc1(rws, to2);
#pragma unroll
                    for (int u = 0; u < 4; ++u) a[u] = ld_sc1(rws, uo + 64 * u);
                    unsigned mx = 0;
#pragma unroll
                    for (int u = 0; u < 3; ++u) mx = umax(umax(mx, t[u].x), t[u].z);
#pragma unroll
                    for (int u = 0; u < 4; ++u) mx = umax(umax(umax(umax(mx, a[u].x), a[u].y), a[u].z), a[u].w);
                    if (__all(mx != POISON) || sp > SPIN_MAX) break;
                }
                STAMP(7);
                float s1 = 0.f, s2 = 0.f;
#pragma unroll
                for (int u = 0; u < 3; ++u) {
                    const unsigned t0 = t[u].x, t1 = t[u].y, t2 = t[u].z, t3 = t[u].w;
                    const float xv = red16(red32(u2f(t0), u2f(t2)), red32(u2f(t1), u2f(t3)));
                    s1 += xv; s2 += xv * xv;
                }
                float s = red32(s1, s2); s = red16(s, s); s = row_sum(s);
                if ((lane & 31) == 0) Sx[half * 4 + (w - 4)] = s;
                BAR();
#pragma unroll
                for (int u = 0; u < 4; ++u)
#pragma unroll
                    for (int n = 0; n < 4; ++n) acc[n] = mfma16(a[u], gf[u][n], acc[n]);
                const v4f o = {sum4(acc[0]), sum4(acc[1]), sum4(acc[2]), sum4(acc[3])};
                if (lane < 16) *(LAS v4f*)(P + w * 64 + l15 * 4) = o;
            }
            BAR();
            LAS u16* gs = (LAS u16*)(L + 8448);
            if (w == 0) {
                LAS float* Q = P + lane;
                const float G = (Q[0] + Q[64]) + (Q[256] + Q[320]), U = (Q[128] + Q[192]) + (Q[384] + Q[448]);
                const unsigned gu = pkbf(rs * G + kg, rs * U + ku);
                const float ag = bflo(gu), au = bfhi(gu);
                constexpr float K0 = -2.f * 1.4426950408889634f * 0.7978845608028654f, K1 = K0 * 0.044715f;
                const float e = __builtin_amdgcn_exp2f(ag * (K0 + K1 * ag * ag));
                const float ge = rb(ag * __builtin_amdgcn_rcpf(1.f + e));
                gs[lane] = (u16)pkbf(ge * au, 0.f);
            }
            BAR();
            STAMP(14);
            {
                const v4u g0 = *(LAS v4u*)(gs + 8 * kq), g1 = *(LAS v4u*)(gs + 32 + 8 * kq);
                v4f y[4] = {};
#pragma unroll
                for (int n = 0; n < 4; ++n) y[n] = mfma16(g0, df[0][n], y[n]);
#pragma unroll
                for (int n = 0; n < 4; ++n) y[n] = mfma16(g1, df[1][n], y[n]);
                const v4u o = {f2u(y[0].x), f2u(y[1].x), f2u(y[2].x), f2u(y[3].x)};
                if (lane < 16) st_sc1(o, rws, WS_H6 + f * 2304 + w * 256 + l15 * 16);
                if (w == 5) {
                    v4f z[4] = {};
#pragma unroll
                    for (int n = 0; n < 4; ++n) z[n] = mfma16(g0, ef[0][n], z[n]);
#pragma unroll
                    for (int n = 0; n < 4; ++n) z[n] = mfma16(g1, ef[1][n], z[n]);
                    const v4u o2 = {f2u(z[0].x), f2u(z[1].x), f2u(z[2].x), f2u(z[3].x)};
                    if (lane < 16) st_sc1(o2, rws, WS_H6 + f * 2304 + 2048 + l15 * 16);
                }
            }
            STAMP(8);
        } else if (b >= 20) {
            const int o6 = b - 20, kg = o6 / 9, nt = o6 - 9 * kg;
            const int head = 8 * kg + w;
            v4u of[4][4];
            {
                const rsrc_t ro = RSRC(wo, 4096 * M * 2);
                const int oo = (128 * head + 8 * kq) * 1152 + nt * 128 + l15 * 8;
                v2u orr[4][8];
#pragma unroll
                for (int u = 0; u < 4; ++u)
#pragma unroll
                    for (int e = 0; e < 8; ++e) orr[u][e] = ld64_nt(ro, oo, (32 * u + e) * 1152);
                SCHED();
#pragma unroll
                for (int u = 0; u < 4; ++u) frag4(orr[u], of[u]);
            }
            STAMP(6);
            float ga0 = 0.f, ga1 = 0.f, c10 = 0.f, c11 = 0.f, x0 = 0.f, x1 = 0.f;
            if (w == 0) {
                const int mo = (64 * nt + 2 * (lane & 31)) * 2;
                const unsigned xr = __builtin_amdgcn_raw_buffer_load_b32(rx, kg == 0 ? mo : OOB, 0, 0);
                unsigned g, c;
                for (int sp = 0;; ++sp) {
                    MEMBAR();
                    g = ld32_sc1(rws, WS_MRB + mo); c = ld32_sc1(rws, WS_MRB + 2 * 1152 + mo);
                    if (__all(g != POISON && c != POISON) || sp > SPIN_MAX) break;
                    __builtin_amdgcn_s_sleep(2);
                }
                ga0 = bflo(g); ga1 = bfhi(g); c10 = bflo(c); c11 = bfhi(c); x0 = bflo(xr); x1 = bfhi(xr);
                PIN(ga0); PIN(ga1); PIN(c10); PIN(c11); PIN(x0); PIN(x1);
            }
            STAMP(13);
            const int co = (l15 < 8 ? WS_CTX + ((head >> 1) * 8 + l15) * 512 + (head & 1) * 256 : OOB) + kq * 16;
            v4u a[4];
            for (int sp = 0;; ++sp) {
                MEMBAR();
#pragma unroll
                for (int u = 0; u < 4; ++u) a[u] = ld_sc1(rws, co + 64 * u);
                unsigned mx = 0;
#pragma unroll
                for (int u = 0; u < 4; ++u) mx = umax(umax(mx, a[u].x), a[u].z);
                if (__all(mx != POISON) || sp > SPIN_MAX) break;
            }
            STAMP(7);
            v4f acc[4] = {};
#pragma unroll
            for (int u = 0; u < 4; ++u)
#pragma unroll
                for (int n = 0; n < 4; ++n) acc[n] = mfma16(a[u], of[u][n], acc[n]);
            v2f c; c.x = red16(sum4(acc[0]), sum4(acc[2])); c.y = red16(sum4(acc[1]), sum4(acc[3]));
            if (lane < 32) *(LAS v2f*)(red + w * 64 + l15 * 4 + kq * 2) = c;
            BAR();
            if (tid < 32) {
                LAS v2f* R = (LAS v2f*)red + lane;
                const v2f s = ((R[0] + R[32]) + (R[64] + R[96])) + ((R[128] + R[160]) + (R[192] + R[224]));
                const float t0 = x0 + ga0 * s.x, t1 = x1 + ga1 * s.y;
                st32_sc1(pkbf(t0 * c10, t1 * c11), rws, WS_U5 + kg * 1152 + nt * 128 + lane * 4);
                st64_sc1(v2u{f2u(t0), f2u(t1)}, rws, WS_T5 + kg * 2304 + nt * 256 + lane * 8);
            }
            STAMP(8);
        } else if (b < 18) {
            {
                v4u mw[18]; unsigned mc[18];
#pragma unroll
                for (int i = 0; i < 18; ++i) mc[i] = __builtin_amdgcn_raw_buffer_load_b16(rc, (72 * w + 4 * i + kq) * 2, 0, 0);
#pragma unroll
                for (int i = 0; i < 18; ++i) mw[i] = ld_nt(rmod, (72 * w + 4 * i + kq) * 6912 + 2304 + b * 256 + l15 * 16);
                SCHED();
#pragma unroll
                for (int i = 0; i < 18; ++i) { mc[i] = f2u(silu_bf(bflo(mc[i]))); PIN(mc[i]); }
                ZERO8(acc);
#pragma unroll
                for (int i = 0; i < 18; ++i) dot8z(acc, mw[i], mc[i] >> 16, mc[i]);
                const float b0 = red32(acc[0], acc[4]), b1 = red32(acc[1], acc[5]), b2 = red32(acc[2], acc[6]), b3 = red32(acc[3], acc[7]);
                v2f c; c.x = red16(b0, b2); c.y = red16(b1, b3);
                *(LAS v2f*)(red + w * 128 + l15 * 8 + kq * 2) = c;
                BAR();
                if (tid < 32) {
                    v4f s = *(LAS v4f*)(red + tid * 4);
#pragma unroll
                    for (int k = 1; k < 8; ++k) s += *(LAS v4f*)(red + k * 128 + tid * 4);
                    const int col = 128 * b + 4 * tid;
                    const float one = (col >= 1152 && col < 1728) ? 1.f : 0.f;
                    st64_sc1(v2u{pkbf(rb(s.x) + one, rb(s.y) + one), pkbf(rb(s.z) + one, rb(s.w) + one)}, rws, WS_MRB + col * 2);
                }
                STAMP(6);
            }
            if (w == 0) {
                const int cg = 8 * b + (lane & 7);
                const int m = 4 * cg + 2 * half + (kq & 1);
                v4u t; unsigned gb;
                for (int sp = 0;; ++sp) {
                    MEMBAR();
                    gb = ld32_sc1(rws, WS_MRB + 3 * 1152 + (m & ~1) * 2);
                    t = ld_sc1(rws, WS_T5 + kq * 2304 + cg * 16);
                    if (__all(umax(umax(t.x, t.z), gb) != POISON) || sp > SPIN_MAX) break;
                    __builtin_amdgcn_s_sleep(4);
                }
                const unsigned t0 = t.x, t1 = t.y, t2 = t.z, t3 = t.w;
                float x2 = rb(red16(red32(u2f(t0), u2f(t2)), red32(u2f(t1), u2f(t3))));
                float gsel = (kq & 1) ? bfhi(gb) : bflo(gb);
                PIN(x2); PIN(gsel);
                STAMP(7);
                v4f s;
                for (int sp = 0;; ++sp) {
                    MEMBAR();
                    v4u y[9];
#pragma unroll
                    for (int i = 0; i < 9; ++i) y[i] = ld_sc1(rws, WS_H6 + (8 * i + (lane >> 3)) * 2304 + cg * 16);
                    SCHED();
                    s = __builtin_bit_cast(v4f, y[0]);
#pragma unroll
                    for (int i = 1; i < 9; ++i) s += __builtin_bit_cast(v4f, y[i]);
                    if (__all(s.x == s.x) || sp > SPIN_MAX) break;
                }
                const float yv = rb(add_x8(red16(red32(s.x, s.z), red32(s.y, s.w))));
                const float o = x2 + rb(gsel * yv);
                if ((lane & 8) == 0) out[m] = (u16)pkbf(o, o);
                if (b == 0 && lane == 0) st32_sc1(tag, rws, WS_EPOCH);
                STAMP(8);
            }
        }
    } else {
        const int a = bid - 128;
        constexpr int QOFF = 131072;
        unsigned tag = ld32_sc1(rws, WS_EPOCH);
        {
            const v4u poison = {POISON, POISON, POISON, POISON};
            if (a < 9) {
#pragma unroll
                for (int i = 0; i < 4; ++i) st_sc1(poison, rws, WS_QA + a * 32768 + (i * NT + tid) * 16);
                if (a == 0) { DRAIN(); BAR(); if (tid == 0) st32_sc1(tag + 1u, rws, WS_F1); }
            }
#pragma unroll
            for (int i = 0; i < 4; ++i) st_sc1(poison, rws, WS_INB + a * 32768 + (i * NT + tid) * 16);
            if (w == 7) {
                const rsrc_t rcode = RSRC((const void*)(pc0 & ~255ull), CODE_BYTES);
                unsigned acc = 0;
#pragma unroll
                for (int i = 0; i < (CODE_BYTES + 8191) / 8192; ++i) acc ^= __builtin_amdgcn_raw_buffer_load_b32(rcode, (i * 64 + lane) * 128, 0, 0);
                asm volatile("" :: "v"(acc));
            }
            STAMP(9);
            DRAIN();
            STAMP(10);
            BAR();
            PIN(tag); tag += 1u;
            if (tid == 0) st32_sc1(tag, rws, WS_CL + a * 128);
            if (w == 0) {
                for (int sp = 0;; ++sp) {
                    MEMBAR();
                    const unsigned f = ld32_sc1(rws, WS_F1);
                    if (__all(f == tag) || sp > SPIN_MAX) break;
                    __builtin_amdgcn_s_sleep(2);
                }
                if (A_DELAY) { while (rt() - t_start < (u64)A_DELAY) __builtin_amdgcn_s_sleep(1); }
            }
            BAR();
        }
        STAMP(1);
        {
            const rsrc_t rkv = RSRC(ckv, SK * DC * 2);
            const int g0 = ((w & 3) << 2) | ((w >> 2) & 3), g1 = ((w & 3) << 2) | ((2 + (w >> 2)) & 3);
            const int vo0 = (lane ^ g0) * 16, vo1 = (lane ^ g1) * 16;
#pragma unroll
            for (int i = 0; i < 16; ++i)
                __builtin_amdgcn_raw_ptr_buffer_load_lds(rkv, (LAS void*)(L + (8 * i + w) * 1024), 16, (i & 1) ? vo1 : vo0, (a * 128 + 8 * i + w) * 1024, 0, AUX_NT);
            SCHED();
        }
        STAMP(2);
        const int hpair = a >> 3, cq = a & 7;
        unsigned ad[8];
        if (w < 4) {
            const int r = lane & 31, g = ((r & 3) << 2) | ((r >> 2) & 3);
#pragma unroll
            for (int j = 0; j < 8; ++j) { ad[j] = w * 32768 + r * 1024 + (((2 * j + half) ^ g) * 16); PIN(ad[j]); }
        } else {
            const int blk = kq & 1, q = l15 >> 2, p = lane & 3;
#pragma unroll
            for (int j = 0; j < 8; ++j) {
                const int t = j >> 1, tt = j & 1, g = (q << 2) | ((2 * tt + half) & 3);
                ad[j] = (8 * tt + 4 * half + q) * 1024 + (w - 4) * 256 + (((4 * t + 2 * blk + (p >> 1)) ^ g) * 16) + 8 * (p & 1); PIN(ad[j]);
            }
        }
        unsigned qw[4];
#pragma unroll
        for (int i = 0; i < 4; ++i) { qw[i] = QOFF + (4 * w + i) * 1024 + ((lane ^ ((i << 2) | (w & 3))) * 16); PIN(qw[i]); }
        asm volatile("s_waitcnt vmcnt(%0)" :: "i"(QWAIT) : "memory");
        STAMP(11);
        {
            v4u q[4];
            for (int sp = 0;; ++sp) {
                MEMBAR();
#pragma unroll
                for (int i = 0; i < 4; ++i) q[i] = ld_sc1(rws, WS_QA + (4 * w + i) * 1024 + lane * 16);
                if (__all(umax(umax(q[0].x, q[1].x), umax(q[2].x, q[3].x)) != POISON) || sp > SPIN_MAX) break;
            }
            DRAIN();
            STAMP(12);
#pragma unroll
            for (int i = 0; i < 4; ++i) *(LAS v4u*)(L + qw[i]) = q[i];
        }
        LAS float* mx = (LAS float*)(L + QOFF + 8192);
        LAS float* ls = (LAS float*)(L + QOFF + 8704);
        if (w < 4) {
            v2u vr[2][8];
            {
                const rsrc_t rv = RSRC(wuv, DC * 8192);
                const int vo = (128 * (cq >> 1) + 16 * (cq & 1) + 2 * kq) * 8192 + hpair * 512 + w * 128 + l15 * 8;
#pragma unroll
                for (int u = 0; u < 2; ++u)
#pragma unroll
                    for (int e = 0; e < 8; ++e) vr[u][e] = ld64_nt(rv, vo, (8 * u + (e >> 2) + 32 * (e & 3)) * 8192);
                SCHED();
            }
            BAR();
            STAMP(3); CYC0();
            const int qd = QOFF - w * 32768;
            constexpr int PD = 6;
            v4u kf[32], qf[32];
            v16f acc = {};
#pragma unroll
            for (int s = 0; s < PD; ++s) { kf[s] = *(LAS v4u*)(L + ad[s & 7] + (s >> 3) * 256); qf[s] = *(LAS v4u*)(L + (ad[s & 7] + qd) + (s >> 3) * 256); }
            SCHED();
#pragma unroll
            for (int s = 0; s < 32; ++s) {
                acc = mfma32(kf[s], qf[s], acc);
                if (s + PD < 32) { kf[s + PD] = *(LAS v4u*)(L + ad[(s + PD) & 7] + ((s + PD) >> 3) * 256); qf[s + PD] = *(LAS v4u*)(L + (ad[(s + PD) & 7] + qd) + ((s + PD) >> 3) * 256); }
                SCHED();
            }
            RAWBAR();
            STAMP(13); CYC1();
            float m = -3.0e38f;
#pragma unroll
            for (int i = 0; i < 16; ++i) m = fmaxf(m, acc[i]);
            m = __builtin_ceilf(max32(m, m));
            float p[16];
#pragma unroll
            for (int i = 0; i < 16; ++i) p[i] = __builtin_amdgcn_exp2f(acc[i] - m);
            const v4u f0 = {pkbf(p[0], p[1]), pkbf(p[2], p[3]), pkbf(p[4], p[5]), pkbf(p[6], p[7])};
            const v4u f1 = {pkbf(p[8], p[9]), pkbf(p[10], p[11]), pkbf(p[12], p[13]), pkbf(p[14], p[15])};
            *(LAS v4u*)(L + QOFF + (2 * w) * 1024 + lane * 16) = f0;
            *(LAS v4u*)(L + QOFF + (2 * w + 1) * 1024 + lane * 16) = f1;
            mx[w * 32 + (lane & 31)] = m;
            BAR();
            STAMP(4);
            float l = 0.f;
#pragma unroll
            for (int i = 0; i < 16; ++i) l += p[i];
            l = red32(l, l);
            ls[w * 32 + (lane & 31)] = l;
            BAR();
            if (w == 1 && lane < 16) {
                v2f mk[4], lk[4];
#pragma unroll
                for (int k = 0; k < 4; ++k) { mk[k] = *(LAS v2f*)(mx + 32 * k + 2 * lane); lk[k] = *(LAS v2f*)(ls + 32 * k + 2 * lane); }
                const float m0 = fmaxf(fmaxf(mk[0].x, mk[1].x), fmaxf(mk[2].x, mk[3].x)), m1 = fmaxf(fmaxf(mk[0].y, mk[1].y), fmaxf(mk[2].y, mk[3].y));
                float l0 = 0.f, l1 = 0.f;
#pragma unroll
                for (int k = 0; k < 4; ++k) { l0 += lk[k].x * __builtin_amdgcn_exp2f(mk[k].x - m0); l1 += lk[k].y * __builtin_amdgcn_exp2f(mk[k].y - m1); }
                st_sc1(pack4(m0, m1, l0, l1, tag), rws, WS_ST + a * 256 + lane * 16);
            }
            v4u vf[2][4];
            frag4(vr[0], vf[0]); frag4(vr[1], vf[1]);
            LAS u16* wt = (LAS u16*)(L + QOFF + 12288);
            if (w == 0) {
                v4u s0, s1;
                for (int sp = 0;; ++sp) {
                    MEMBAR();
                    s0 = ld_sc1(rws, WS_ST + lane * 256 + hpair * 16); s1 = ld_sc1(rws, WS_ST + (64 + lane) * 256 + hpair * 16);
                    if (__all(s0.w == tag && s1.w == tag) || sp > SPIN_MAX) break;
                    __builtin_amdgcn_s_sleep(1);
                }
                STAMP(6);
                const v4f sa = unpack4(s0), sb = unpack4(s1);
                float mm = max32(fmaxf(sa.x, sb.x), fmaxf(sa.y, sb.y));
                mm = row_max(max16(mm));
                auto sw = __builtin_amdgcn_permlane32_swap(f2u(mm), f2u(mm), false, false);
                const unsigned M0 = sw[0], M1 = sw[1];
                const float e00 = rb(__builtin_amdgcn_exp2f(sa.x - u2f(M0))), e01 = rb(__builtin_amdgcn_exp2f(sa.y - u2f(M1)));
                const float e10 = rb(__builtin_amdgcn_exp2f(sb.x - u2f(M0))), e11 = rb(__builtin_amdgcn_exp2f(sb.y - u2f(M1)));
                float ll = red32(e00 * sa.z + e10 * sb.z, e01 * sa.w + e11 * sb.w);
                ll = red16(ll, ll); ll = __builtin_amdgcn_rcpf(row_sum(ll));
                ((LAS float*)(L + QOFF + 12800))[half] = ll;
                wt[lane] = (u16)(f2u(e00) >> 16); wt[64 + lane] = (u16)(f2u(e10) >> 16); wt[128 + lane] = (u16)(f2u(e01) >> 16); wt[192 + lane] = (u16)(f2u(e11) >> 16);
            }
            const int io = WS_INB + a * 32768 + (32 * w + 8 * kq) * 256 + l15 * 16;
            BAR();
            v4u wa = *(LAS v4u*)(wt + (lane & 1) * 128 + 32 * w + 8 * kq);
            float rl = ((LAS float*)(L + QOFF + 12800))[w >> 1];
            STAMP(14);
            v4u d[8];
            for (int sp = 0;; ++sp) {
                MEMBAR();
#pragma unroll
                for (int e = 0; e < 8; ++e) d[e] = ld_sc1(rws, io + e * 256);
                const unsigned mxp = umax(umax(umax(d[0].x, d[1].x), umax(d[2].x, d[3].x)), umax(umax(d[4].x, d[5].x), umax(d[6].x, d[7].x)));
                if (__all(mxp != POISON) || sp > SPIN_MAX) break;
            }
            STAMP(7);
            float lat[2][4];
#pragma unroll
            for (int t = 0; t < 4; ++t) {
                v4u lo, hi;
#pragma unroll
                for (int p2 = 0; p2 < 4; ++p2) { const unsigned u0 = d[2 * p2][t], u1 = d[2 * p2 + 1][t]; lo[p2] = perm(u1, u0, 0x05040100u); hi[p2] = perm(u1, u0, 0x07060302u); }
                const v4f z = {0.f, 0.f, 0.f, 0.f};
                lat[0][t] = mfma16(wa, lo, z).x; lat[1][t] = mfma16(wa, hi, z).y;
            }
            LAS unsigned char* la = L + QOFF + 13312;
            {
                const v2u o0 = {pkbf(lat[0][0], lat[0][1]), pkbf(lat[0][2], lat[0][3])}, o1 = {pkbf(lat[1][0], lat[1][1]), pkbf(lat[1][2], lat[1][3])};
                if (lane < 16) { *(LAS v2u*)(la + w * 128 + l15 * 8) = o0; *(LAS v2u*)(la + 512 + w * 128 + l15 * 8) = o1; }
            }
            BAR();
            {
                LAS unsigned char* lr = la + (w >> 1) * 512 + (lane & 3) * 128 + kq * 16;
                const v4u a0 = *(LAS v4u*)lr, a1 = *(LAS v4u*)(lr + 64);
                v4f y[4] = {};
#pragma unroll
                for (int n = 0; n < 4; ++n) y[n] = mfma16(a0, vf[0][n], y[n]);
#pragma unroll
                for (int n = 0; n < 4; ++n) y[n] = mfma16(a1, vf[1][n], y[n]);
                const v2u o = {pkbf(sum4(y[0]) * rl, sum4(y[1]) * rl), pkbf(sum4(y[2]) * rl, sum4(y[3]) * rl)};
                if (lane < 16) st64_sc1(o, rws, WS_CTX + a * 512 + w * 128 + l15 * 8);
            }
            STAMP(8);
        } else {
            BAR();
            const int v = w - 4;
            const int col = lane & 31;
            int ob = WS_INB + ((2 * half) * 8 + 2 * v + (col >> 4)) * 32768 + a * 256 + (col & 15) * 16;
            PIN(ob);
            RAWBAR();
            v4s vb[4][16];
#define VREAD(t) _Pragma("unroll") for (int k = 0; k < 16; ++k) vb[t][k] = __builtin_amdgcn_ds_read_tr16_b64_v4i16((LAS v4s*)(L + (ad[2 * (t) + (k & 1)] + (k >> 3) * 65536) + ((k >> 1) & 3) * 16384))
            VREAD(0); VREAD(1);
            BAR();
            STAMP(4); CYC0();
            v4u pa[8];
            const float m0 = mx[col], m1 = mx[32 + col], m2 = mx[64 + col], m3 = mx[96 + col];
#pragma unroll
            for (int ks = 0; ks < 8; ++ks) pa[ks] = *(LAS v4u*)(L + QOFF + ks * 1024 + lane * 16);
            SCHED();
            const float mm = fmaxf(fmaxf(fmaxf(m0, m1), m2), m3);
            const float mk[4] = {m0, m1, m2, m3};
            unsigned pk[8][4];
            v16f acc[4];
#pragma unroll
            for (int t = 0; t < 4; ++t) {
#pragma unroll
                for (int ks = 0; ks < 8; ++ks) {
                    if (t == 0) {
                        const unsigned sub = (unsigned)fminf(mm - mk[ks >> 1], 255.f) * 0x00800080u;
#pragma unroll
                        for (int i = 0; i < 4; ++i) { const unsigned u = pa[ks][i]; pa[ks][i] = __builtin_bit_cast(unsigned, __builtin_elementwise_sub_sat(__builtin_bit_cast(v2us, u), __builtin_bit_cast(v2us, sub))); }
                    }
                    const v4u bv = __builtin_bit_cast(v4u, __builtin_shufflevector(vb[t][2 * ks], vb[t][2 * ks + 1], 0, 1, 2, 3, 4, 5, 6, 7));
                    const v16f z = {};
                    acc[t] = mfma32(pa[ks], bv, ks ? acc[t] : z);
                    if (t < 2) {
                        vb[t + 2][2 * ks] = __builtin_amdgcn_ds_read_tr16_b64_v4i16((LAS v4s*)(L + (ad[2 * (t + 2)] + (ks >> 2) * 65536) + (ks & 3) * 16384));
                        vb[t + 2][2 * ks + 1] = __builtin_amdgcn_ds_read_tr16_b64_v4i16((LAS v4s*)(L + (ad[2 * (t + 2) + 1] + (ks >> 2) * 65536) + (ks & 3) * 16384));
                    }
                    if (t > 0 && ks >= 2 && ks < 6) {
                        pk[2 * (ks - 2)][t - 1] = pkbf(acc[t - 1][4 * (ks - 2)], acc[t - 1][4 * (ks - 2) + 1]);
                        pk[2 * (ks - 2) + 1][t - 1] = pkbf(acc[t - 1][4 * (ks - 2) + 2], acc[t - 1][4 * (ks - 2) + 3]);
                    }
                    if (t == 0 && ks == 5) RAWBAR();
                    SCHED();
                }
            }
            STAMP(13); CYC1();
#pragma unroll
            for (int e = 0; e < 8; ++e) {
                const v4u o = {pk[e][0], pk[e][1], pk[e][2], pkbf(acc[3][2 * e], acc[3][2 * e + 1])};
                __builtin_amdgcn_raw_buffer_store_b128(o, rws, ob, (4 * (e >> 1) + (e & 1)) * 8 * 32768, AUX_SC1);
            }
            STAMP(5);
        }
    }
    STAMP_FLUSH();
}

extern "C" void kernel_launch(void* const* d_in, const int* in_sizes, int n_in, void* d_out, int out_size, void* d_ws, size_t ws_size, hipStream_t stream) {
    fused_kernel<<<256, NT, 0, stream>>>((const u16*)d_in[0], (const u16*)d_in[1], (const u16*)d_in[2], (const u16*)d_in[3], (const u16*)d_in[4], (const u16*)d_in[5],
                                         (const u16*)d_in[7], (const u16*)d_in[8], (const u16*)d_in[9], (const u16*)d_in[10], (const u16*)d_in[11], (u16*)d_out, (unsigned char*)d_ws);
}
static inline int stamp_group(int b) { return b >= 128 ? 0 : (b >= 56 ? 1 : (b >= 20 ? 2 : (b < 18 ? 3 : 4))); }
```

```cpp
#include <hip/hip_runtime.h>
#include <hip/hip_bf16.h>

#define STAMPS 0
#define LATE_DELAY 400
#define A_DELAY 0
#define QWAIT 2

typedef unsigned short u16;
typedef unsigned long long u64;
typedef unsigned v4u __attribute__((ext_vector_type(4)));
typedef unsigned v2u __attribute__((ext_vector_type(2)));
typedef float v4f __attribute__((ext_vector_type(4)));
typedef float v2f __attribute__((ext_vector_type(2)));
typedef float v16f __attribute__((ext_vector_type(16)));
typedef short v4s __attribute__((ext_vector_type(4)));
typedef unsigned short v2us __attribute__((ext_vector_type(2)));
typedef __bf16 v2bf __attribute__((ext_vector_type(2)));
typedef __bf16 v8bf __attribute__((ext_vector_type(8)));
#define LAS __attribute__((address_space(3)))
#define GAS __attribute__((address_space(1)))
typedef __amdgpu_buffer_rsrc_t rsrc_t;

constexpr int M = 576, DC = 512, SK = 16384, FF = 4608;
constexpr int NT = 512;
constexpr int CODE_BYTES = 24320;
constexpr int SPIN_MAX = 1 << 16;
constexpr unsigned POISON = 0xFFFFFFFFu;
constexpr int OOB = 0x40000000;
constexpr int WS_EPOCH = 0, WS_F1 = 256;
constexpr int WS_CL = 4096;
constexpr int WS_H1 = WS_CL + 128 * 128;
constexpr int WS_HQ = WS_H1 + 4 * 288 * 16;
constexpr int WS_ST = WS_HQ + 32 * 4 * 512;
constexpr int WS_QA = 256 * 1024;
constexpr int WS_PZ = WS_QA + 32768;
constexpr int WS_CTX = WS_PZ;
constexpr int WS_U5 = WS_CTX + 65536;
constexpr int WS_T5 = WS_U5 + 4608;
constexpr int WS_MRB = WS_T5 + 9216;
constexpr int WS_H6 = WS_MRB + 4608;
constexpr int WS_INB = 1 << 20;
constexpr int WS_STAMP = WS_INB + (4 << 20);
constexpr int WS_BYTES = WS_STAMP + 256 * 8 * 16 * 4;
static_assert(WS_ST + 32768 <= WS_QA && WS_H6 + 72 * 2304 <= WS_PZ + 8 * 32768 && WS_PZ + 8 * 32768 <= WS_INB, "ws map");
static_assert(WS_H1 % 128 == 0 && WS_HQ % 128 == 0 && WS_ST % 128 == 0 && WS_U5 % 128 == 0 && WS_T5 % 128 == 0 && WS_MRB % 128 == 0 && WS_H6 % 128 == 0, "line aligned");

#define RSRC(p, bytes) __builtin_amdgcn_make_buffer_rsrc((void*)(p), 0, (bytes), 0x00020000)
#define AUX_NT 2
#define AUX_SC1 16
__device__ __forceinline__ v4u ld_nt(rsrc_t r, int off) { return __builtin_amdgcn_raw_buffer_load_b128(r, off, 0, AUX_NT); }
__device__ __forceinline__ v2u ld64_nt(rsrc_t r, int off, int so) { return __builtin_amdgcn_raw_buffer_load_b64(r, off, so, AUX_NT); }
__device__ __forceinline__ v4u ld_sc1(rsrc_t r, int off) { return __builtin_amdgcn_raw_buffer_load_b128(r, off, 0, AUX_SC1); }
__device__ __forceinline__ unsigned ld32_sc1(rsrc_t r, int off) { return __builtin_amdgcn_raw_buffer_load_b32(r, off, 0, AUX_SC1); }
__device__ __forceinline__ void st_sc1(v4u v, rsrc_t r, int off) { __builtin_amdgcn_raw_buffer_store_b128(v, r, off, 0, AUX_SC1); }
__device__ __forceinline__ void st64_sc1(v2u v, rsrc_t r, int off) { __builtin_amdgcn_raw_buffer_store_b64(v, r, off, 0, AUX_SC1); }
__device__ __forceinline__ void st32_sc1(unsigned v, rsrc_t r, int off) { __builtin_amdgcn_raw_buffer_store_b32(v, r, off, 0, AUX_SC1); }
#define MEMBAR() asm volatile("" ::: "memory")
#define DRAIN() asm volatile("s_waitcnt vmcnt(0)" ::: "memory")
#define SCHED() __builtin_amdgcn_sched_barrier(0)
#define PIN(x) asm volatile("" : "+v"(x))
#define BAR() do { asm volatile("s_waitcnt lgkmcnt(0)\n\ts_barrier" ::: "memory"); } while (0)
#define RAWBAR() do { asm volatile("s_barrier" ::: "memory"); } while (0)

__device__ __forceinline__ float u2f(unsigned u) { return __builtin_bit_cast(float, u); }
__device__ __forceinline__ unsigned f2u(float f) { return __builtin_bit_cast(unsigned, f); }
__device__ __forceinline__ float bflo(unsigned u) { return u2f(u << 16); }
__device__ __forceinline__ float bfhi(unsigned u) { return u2f(u & 0xFFFF0000u); }
__device__ __forceinline__ unsigned pkbf(float lo, float hi) { v2f p = {lo, hi}; v2bf q = __builtin_convertvector(p, v2bf); return __builtin_bit_cast(unsigned, q); }
__device__ __forceinline__ float rb(float f) { return bflo(pkbf(f, f)); }
__device__ __forceinline__ float dot2(unsigned a, unsigned b, float c) { return __builtin_amdgcn_fdot2_f32_bf16(__builtin_bit_cast(v2bf, a), __builtin_bit_cast(v2bf, b), c, false); }
__device__ __forceinline__ unsigned perm(unsigned hi, unsigned lo, unsigned sel) { return __builtin_amdgcn_perm(hi, lo, sel); }
__device__ __forceinline__ unsigned umax(unsigned a, unsigned b) { return a > b ? a : b; }
__device__ __forceinline__ v4f mfma16(v4u a, v4u b, v4f c) { return __builtin_amdgcn_mfma_f32_16x16x32_bf16(__builtin_bit_cast(v8bf, a), __builtin_bit_cast(v8bf, b), c, 0, 0, 0); }
__device__ __forceinline__ v16f mfma32(v4u a, v4u b, v16f c) { return __builtin_amdgcn_mfma_f32_32x32x16_bf16(__builtin_bit_cast(v8bf, a), __builtin_bit_cast(v8bf, b), c, 0, 0, 0); }
__device__ __forceinline__ float sum4(v4f a) { return (a.x + a.z) + (a.y + a.w); }
__device__ __forceinline__ v4u pack4(float a, float b, float c, float d, unsigned tag) {
    const unsigned ud = f2u(d);
    v4u g; g.x = perm(f2u(a), ud, 0x07060501u); g.y = perm(f2u(b), ud, 0x07060502u); g.z = perm(f2u(c), ud, 0x07060503u); g.w = tag; return g;
}
__device__ __forceinline__ v4f unpack4(v4u g) {
    const unsigned gx = g.x, gy = g.y, gz = g.z;
    const unsigned t = perm(gy, gx, 0x0c04000cu);
    const unsigned d = perm(gz, t, 0x04020100u);
    v4f r = {u2f(gx & 0xFFFFFF00u), u2f(gy & 0xFFFFFF00u), u2f(gz & 0xFFFFFF00u), u2f(d)}; return r;
}
__device__ __forceinline__ float red32(float a, float b) { auto r = __builtin_amdgcn_permlane32_swap(f2u(a), f2u(b), false, false); const unsigned x = r[0], y = r[1]; return u2f(x) + u2f(y); }
__device__ __forceinline__ float red16(float a, float b) { auto r = __builtin_amdgcn_permlane16_swap(f2u(a), f2u(b), false, false); const unsigned x = r[0], y = r[1]; return u2f(x) + u2f(y); }
__device__ __forceinline__ float max32(float a, float b) { auto r = __builtin_amdgcn_permlane32_swap(f2u(a), f2u(b), false, false); const unsigned x = r[0], y = r[1]; return fmaxf(u2f(x), u2f(y)); }
__device__ __forceinline__ float max16(float a) { auto r = __builtin_amdgcn_permlane16_swap(f2u(a), f2u(a), false, false); const unsigned x = r[0], y = r[1]; return fmaxf(u2f(x), u2f(y)); }
template <int CTRL> __device__ __forceinline__ float dpp(float v) { return u2f((unsigned)__builtin_amdgcn_update_dpp(0, (int)f2u(v), CTRL, 0xf, 0xf, true)); }
__device__ __forceinline__ float add_x8(float v) { return v + dpp<0x128>(v); }
__device__ __forceinline__ float add_r4(float v) { return v + dpp<0x124>(v); }
__device__ __forceinline__ float add_x2(float v) { return v + dpp<0x4E>(v); }
__device__ __forceinline__ float add_x1(float v) { return v + dpp<0xB1>(v); }
__device__ __forceinline__ float row_sum(float v) { return add_r4(add_x8(add_x2(add_x1(v)))); }
__device__ __forceinline__ float row_max(float v) { v = fmaxf(v, dpp<0xB1>(v)); v = fmaxf(v, dpp<0x4E>(v)); v = fmaxf(v, dpp<0x128>(v)); return fmaxf(v, dpp<0x124>(v)); }
__device__ __forceinline__ float wave_sum(float v) { v = row_sum(v); v = red16(v, v); return red32(v, v); }
__device__ __forceinline__ void interleave(v4u& a, v4u& b) {
    const unsigned a0 = a.x, a1 = a.y, a2 = a.z, a3 = a.w, b0 = b.x, b1 = b.y, b2 = b.z, b3 = b.w;
    a.x = perm(b0, a0, 0x05040100u); a.y = perm(b0, a0, 0x07060302u); a.z = perm(b1, a1, 0x05040100u); a.w = perm(b1, a1, 0x07060302u);
    b.x = perm(b2, a2, 0x05040100u); b.y = perm(b2, a2, 0x07060302u); b.z = perm(b3, a3, 0x05040100u); b.w = perm(b3, a3, 0x07060302u);
}
__device__ __forceinline__ void frag4(const v2u* r, v4u* f) {
    unsigned o[4][4];
#pragma unroll
    for (int p = 0; p < 4; ++p) {
        const unsigned ax = r[2 * p].x, ay = r[2 * p].y, bx = r[2 * p + 1].x, by = r[2 * p + 1].y;
        o[0][p] = perm(bx, ax, 0x05040100u); o[1][p] = perm(bx, ax, 0x07060302u); o[2][p] = perm(by, ay, 0x05040100u); o[3][p] = perm(by, ay, 0x07060302u);
    }
#pragma unroll
    for (int n = 0; n < 4; ++n) { f[n] = v4u{o[n][0], o[n][1], o[n][2], o[n][3]}; PIN(f[n]); }
}
__device__ __forceinline__ void dot8(float (&acc)[8], const v4u& a, const v4u& b, unsigned xp) {
    const unsigned a0 = a.x, a1 = a.y, a2 = a.z, a3 = a.w, b0 = b.x, b1 = b.y, b2 = b.z, b3 = b.w;
    acc[0] = dot2(a0, xp, acc[0]); acc[1] = dot2(a1, xp, acc[1]); acc[2] = dot2(a2, xp, acc[2]); acc[3] = dot2(a3, xp, acc[3]);
    acc[4] = dot2(b0, xp, acc[4]); acc[5] = dot2(b1, xp, acc[5]); acc[6] = dot2(b2, xp, acc[6]); acc[7] = dot2(b3, xp, acc[7]);
}
__device__ __forceinline__ void dot8z(float (&acc)[8], const v4u& a, unsigned slo, unsigned shi) {
    const unsigned a0 = a.x, a1 = a.y, a2 = a.z, a3 = a.w;
    acc[0] = dot2(a0, slo, acc[0]); acc[1] = dot2(a0, shi, acc[1]); acc[2] = dot2(a1, slo, acc[2]); acc[3] = dot2(a1, shi, acc[3]);
    acc[4] = dot2(a2, slo, acc[4]); acc[5] = dot2(a2, shi, acc[5]); acc[6] = dot2(a3, slo, acc[6]); acc[7] = dot2(a3, shi, acc[7]);
}
#define ZERO8(acc) float acc[8] = {0.f, 0.f, 0.f, 0.f, 0.f, 0.f, 0.f, 0.f}; asm volatile("" : "+v"(acc[0]), "+v"(acc[1]), "+v"(acc[2]), "+v"(acc[3]), "+v"(acc[4]), "+v"(acc[5]), "+v"(acc[6]), "+v"(acc[7]))
__device__ __forceinline__ float red8cols(const float (&a)[8], int lane) {
    const float b0 = red32(a[0], a[4]), b1 = red32(a[1], a[5]), b2 = red32(a[2], a[6]), b3 = red32(a[3], a[7]);
    const float c0 = red16(b0, b2), c1 = red16(b1, b3);
    const float d0 = add_x8(c0), d1 = add_x8(c1);
    return (lane & 8) ? d1 : d0;
}
__device__ __forceinline__ float silu_bf(float v) { return rb(v * __builtin_amdgcn_rcpf(1.f + __expf(-v))); }
__device__ __forceinline__ u64 rt() { return __builtin_amdgcn_s_memrealtime(); }
#define STAMP(i)
#define STAMP_FLUSH()
#define CYC0()
#define CYC1()

constexpr int WS_T0 = 2048;
__global__ void k_first(unsigned char* ws) { if (threadIdx.x == 0) *(GAS unsigned*)(ws + WS_T0) = (unsigned)rt(); }
__global__ void k_last(unsigned char* ws) { const unsigned t0 = *(volatile GAS unsigned*)(ws + WS_T0); while ((unsigned)rt() - t0 < 6000u) __builtin_amdgcn_s_sleep(2); }
__global__ void __launch_bounds__(NT, 2) fused_kernel(const u16* __restrict__ x, const u16* __restrict__ ckv, const u16* __restrict__ wq, const u16* __restrict__ wuk,
                                                      const u16* __restrict__ wuv, const u16* __restrict__ wo, const u16* __restrict__ wg, const u16* __restrict__ wu,
                                                      const u16* __restrict__ wd, const u16* __restrict__ cc, const u16* __restrict__ wmod, u16* __restrict__ out,
                                                      unsigned char* __restrict__ ws) {
    __shared__ __attribute__((aligned(16))) unsigned char smem[163840];
    LAS unsigned char* L = (LAS unsigned char*)smem;
    u64 pc0; asm volatile("s_getpc_b64 %0" : "=s"(pc0));
    const u64 t_start = rt();
    asm volatile("" : "+s"(x), "+s"(ckv), "+s"(wq), "+s"(wuk), "+s"(wuv), "+s"(wo), "+s"(wg), "+s"(wu), "+s"(wd), "+s"(cc), "+s"(wmod), "+s"(out), "+s"(ws));
    const int bid = blockIdx.x, tid = threadIdx.x, lane = tid & 63, w = __builtin_amdgcn_readfirstlane(tid >> 6);
    const rsrc_t rws = RSRC(ws, WS_BYTES);
    if (bid == 0 && tid == 0) *(GAS unsigned*)(ws + WS_T0) = (unsigned)t_start;
    const rsrc_t rx = RSRC(x, M * 2);
    const int l15 = lane & 15, kq = lane >> 4, half = lane >> 5;

    if (__builtin_expect(bid < 128, 1)) {
        const int b = bid, hh = b >> 2, mj = b & 3;
        const rsrc_t rmod = RSRC(wmod, M * 6 * M * 2), rc = RSRC(cc, M * 2);
        LAS float* red = (LAS float*)L;
        LAS unsigned* hp = (LAS unsigned*)(L + 20480);
        const bool doS1 = b < 72;
        unsigned tag;
        if (__builtin_expect(w < 4, 1)) {
            const int kp = b / 18, nt = b - 18 * kp;
            const int r1 = 144 * kp + 36 * w + (lane >> 3);
            const int co = doS1 ? r1 * 2 : OOB, wo1 = doS1 ? r1 * 6912 + nt * 128 + (lane & 7) * 16 : OOB;
            unsigned s1c[5]; v4u s1w[5], q2[9];
#pragma unroll
            for (int i = 0; i < 5; ++i) s1c[i] = __builtin_amdgcn_raw_buffer_load_b16(rc, (i < 4 || lane < 32) ? co + 16 * i : OOB, 0, 0);
#pragma unroll
            for (int i = 0; i < 5; ++i) s1w[i] = ld_nt(rmod, (i < 4 || lane < 32) ? wo1 + 8 * i * 6912 : OOB);
            tag = ld32_sc1(rws, WS_EPOCH);
            const rsrc_t rq = RSRC(wq, M * 8192);
            const int qo = (144 * mj + 36 * w) * 8192 + hh * 256 + l15 * 16;
#pragma unroll
            for (int i = 0; i < 8; ++i) q2[i] = ld_nt(rq, qo + (8 * (i >> 1) + 2 * kq + (i & 1)) * 8192);
            q2[8] = ld_nt(rq, qo + (32 + kq) * 8192);
            SCHED();
            STAMP(9);
            if (doS1) {
                unsigned slo[5], shi[5];
#pragma unroll
                for (int i = 0; i < 5; ++i) { shi[i] = f2u(silu_bf(bflo(s1c[i]))); slo[i] = shi[i] >> 16; PIN(shi[i]); PIN(slo[i]); }
                ZERO8(acc);
                SCHED();
#pragma unroll
                for (int i = 0; i < 5; ++i) dot8z(acc, s1w[i], slo[i], shi[i]);
                red[w * 64 + (lane & 7) * 8 + (lane >> 3)] = red8cols(acc, lane);
                STAMP(10);
                BAR();
                if (tid < 16) {
                    const v4f s = (*(LAS v4f*)(red + tid * 4) + *(LAS v4f*)(red + 64 + tid * 4)) + (*(LAS v4f*)(red + 128 + tid * 4) + *(LAS v4f*)(red + 192 + tid * 4));
                    st_sc1(pack4(s.x, s.y, s.z, s.w, tag + 1u), rws, WS_H1 + kp * 4608 + (nt * 16 + tid) * 16);
                }
                STAMP(1);
                BAR();
            }
            PIN(tag); tag += 1u;
#pragma unroll
            for (int i = 0; i < 4; ++i) { interleave(q2[2 * i], q2[2 * i + 1]); PIN(q2[2 * i]); PIN(q2[2 * i + 1]); }
            PIN(q2[8]);
            ZERO8(acc);
            STAMP(12);
            BAR();
            STAMP(2);
            {
                const unsigned h0 = hp[18 * w + kq], h1 = hp[18 * w + 4 + kq], h2 = hp[18 * w + 8 + kq], h3 = hp[18 * w + 12 + kq];
                const unsigned hs = ((LAS u16*)hp)[36 * w + 32 + kq];
                dot8(acc, q2[0], q2[1], h0); dot8(acc, q2[2], q2[3], h1); dot8(acc, q2[4], q2[5], h2); dot8(acc, q2[6], q2[7], h3);
                dot8z(acc, q2[8], hs, hs << 16);
                const float b0 = red32(acc[0], acc[4]), b1 = red32(acc[1], acc[5]), b2 = red32(acc[2], acc[6]), b3 = red32(acc[3], acc[7]);
                v2f c; c.x = red16(b0, b2); c.y = red16(b1, b3);
                *(LAS v2f*)(red + w * 128 + l15 * 8 + kq * 2) = c;
            }
            BAR();
            if (tid < 32) {
                const v4f s = (*(LAS v4f*)(red + tid * 4) + *(LAS v4f*)(red + 128 + tid * 4)) + (*(LAS v4f*)(red + 256 + tid * 4) + *(LAS v4f*)(red + 384 + tid * 4));
                st_sc1(pack4(s.x, s.y, s.z, s.w, tag), rws, WS_HQ + b * 512 + tid * 16);
            }
            STAMP(3);
            if (w == 1) {
                const int of = lane == 0 ? WS_F1 : (lane < 9 ? WS_CL + lane * 128 : WS_CL + b * 128);
                for (int sp = 0;; ++sp) {
                    MEMBAR();
                    const unsigned f = ld32_sc1(rws, of);
                    if (__all(f == tag) || sp > SPIN_MAX) break;
                }
            }
            BAR();
            if (tid < 16) st_sc1(*(LAS v4u*)(L + 24576 + tid * 16), rws, WS_QA + hh * 1024 + mj * 256 + tid * 16);
            STAMP(5);
        } else {
            tag = ld32_sc1(rws, WS_EPOCH);
            float npair0 = 0.f, npair1 = 0.f;
            const int j = (lane & 31) < 18 ? (lane & 31) : 17;
            if (w < 6) {
                const v4u xa = __builtin_amdgcn_raw_buffer_load_b128(rx, lane * 16, 0, 0);
                const unsigned xb = __builtin_amdgcn_raw_buffer_load_b32(rx, 1024 + lane * 4, 0, 0);
                const int m0 = 144 * mj + 4 * (18 * (w - 4) + j) + 2 * half;
                const unsigned xm = __builtin_amdgcn_raw_buffer_load_b32(rx, m0 * 2, 0, 0);
                if (doS1) { BAR(); BAR(); }
                const unsigned a0 = xa.x, a1 = xa.y, a2 = xa.z, a3 = xa.w;
                float v[10] = {bflo(a0), bfhi(a0), bflo(a1), bfhi(a1), bflo(a2), bfhi(a2), bflo(a3), bfhi(a3), bflo(xb), bfhi(xb)};
                float s = 0.f;
#pragma unroll
                for (int i = 0; i < 10; ++i) s += v[i];
                const float mean = wave_sum(s) * (1.f / M);
                float q = 0.f;
#pragma unroll
                for (int i = 0; i < 8; ++i) q += (v[i] - mean) * (v[i] - mean);
                if (lane < 32) q += (v[8] - mean) * (v[8] - mean) + (v[9] - mean) * (v[9] - mean);
                const float var = rb(wave_sum(q) * (1.f / M));
                const float rden = __builtin_amdgcn_rcpf(rb(__builtin_amdgcn_sqrtf(rb(var + 1e-5f)))), mb = rb(mean);
                npair0 = rb(rb(bflo(xm) - mb) * rden); npair1 = rb(rb(bfhi(xm) - mb) * rden);
            } else if (doS1) { BAR(); BAR(); }
            v4u k3[8];
            {
                const rsrc_t rk = RSRC(wuk, DC * 8192);
                const int ko = (128 * mj + 32 * (w - 4) + kq) * 8192 + hh * 256 + l15 * 16;
#pragma unroll
                for (int i = 0; i < 8; ++i) k3[i] = ld_nt(rk, ko + 4 * i * 8192);
                SCHED();
            }
            PIN(tag); tag += 1u;
            PIN(npair0); PIN(npair1);
            STAMP(11);
            if (w < 6) {
                const int cg = 36 * mj + 18 * (w - 4) + j;
                const int o = WS_H1 + (2 * half) * 4608 + cg * 16;
                v4u g0, g1, g2, g3;
                for (int sp = 0;; ++sp) {
                    MEMBAR();
                    g0 = ld_sc1(rws, o); g1 = ld_sc1(rws, o + 4608); g2 = ld_sc1(rws, o + 2304); g3 = ld_sc1(rws, o + 2304 + 4608);
                    const bool ok = g0.w == tag && g1.w == tag && g2.w == tag && g3.w == tag;
                    if (__all(ok) || sp > SPIN_MAX) break;
                }
                STAMP(12);
                const v4f sa = unpack4(g0) + unpack4(g1), ca = unpack4(g2) + unpack4(g3);
                const unsigned sp = pkbf(red32(sa.x, sa.z), red32(sa.y, sa.w)), cp = pkbf(red32(ca.x, ca.z), red32(ca.y, ca.w));
                const unsigned c1 = pkbf(1.f + bflo(cp), 1.f + bfhi(cp));
                const unsigned t1 = pkbf(npair0 * bflo(c1), npair1 * bfhi(c1));
                hp[2 * (18 * (w - 4) + j) + half] = pkbf(bflo(t1) + bflo(sp), bfhi(t1) + bfhi(sp));
            }
            BAR();
            BAR();
            {
                const int o = WS_HQ + (hh * 4 + kq) * 512 + l15 * 32;
                v4u g0, g1;
                for (int sp = 0;; ++sp) {
                    MEMBAR();
                    g0 = ld_sc1(rws, o); g1 = ld_sc1(rws, o + 16);
                    if (__all(g0.w == tag && g1.w == tag) || sp > SPIN_MAX) break;
                }
                STAMP(4);
                const v4f qa = unpack4(g0), qb = unpack4(g1);
                const float b0 = red32(qa.x, qb.x), b1 = red32(qa.y, qb.y), b2 = red32(qa.z, qb.z), b3 = red32(qa.w, qb.w);
                const unsigned pp = pkbf(red16(b0, b2), red16(b1, b3));
                auto s16 = __builtin_amdgcn_permlane16_swap(pp, pp, false, false);
                const unsigned pe = s16[0], po = s16[1];
                auto se = __builtin_amdgcn_permlane32_swap(pe, pe, false, false);
                auto so = __builtin_amdgcn_permlane32_swap(po, po, false, false);
                const unsigned p0 = se[0], p2 = se[1], p1 = so[0], p3 = so[1];
                float a[8];
#pragma unroll
                for (int i = 0; i < 8; ++i) { const unsigned k0 = k3[i].x, k1 = k3[i].y, k2 = k3[i].z, k3w = k3[i].w; a[i] = dot2(k3w, p3, dot2(k2, p2, dot2(k1, p1, dot2(k0, p0, 0.f)))); }
                float c[4];
#pragma unroll
                for (int i = 0; i < 4; ++i) { const float u0 = add_x8(a[i]), u1 = add_x8(a[i + 4]); c[i] = (lane & 8) ? u1 : u0; }
                const float e0 = add_x2(c[0]), e1 = add_x2(c[1]), e2 = add_x2(c[2]), e3 = add_x2(c[3]);
                const float f0 = (lane & 2) ? e2 : e0, f1 = (lane & 2) ? e3 : e1;
                const float g0f = add_x1(f0), g1f = add_x1(f1);
                float r = (lane & 1) ? g1f : g0f;
                r += dpp<0x141>(dpp<0x1B>(r));
                r *= 0.08838834764831845f * 1.4426950408889634f;
                LAS u16* qo = (LAS u16*)(L + 24576);
                const int i = ((lane >> 1) & 4) | (lane & 3);
                qo[32 * (w - 4) + 4 * i + kq] = (u16)pkbf(r, r);
            }
            BAR();
            STAMP(5);
        }
        if (LATE_DELAY) { while (rt() - t_start < (u64)LATE_DELAY) __builtin_amdgcn_s_sleep(1); }
        if (b >= 56) {
            const int f = b - 56;
            const int ks0 = 9 * (w & 1) + (w < 4 ? 0 : 5);
            v4u gf[5][4], df[2][4], ef[2][4];
            {
                const rsrc_t rg = RSRC((w & 2) ? wu : wg, M * FF * 2), rd = RSRC(wd, M * FF * 2);
                const int go = (32 * ks0 + 8 * kq) * 9216 + f * 128 + l15 * 8;
                const int dofs = (64 * f + 8 * kq) * 1152 + l15 * 8;
                v2u gr[5][8], dr[2][8], er[2][8];
#pragma unroll
                for (int u = 0; u < 4; ++u)
#pragma unroll
                    for (int e = 0; e < 8; ++e) gr[u][e] = ld64_nt(rg, go, (32 * u + e) * 9216);
#pragma unroll
                for (int u = 0; u < 2; ++u)
#pragma unroll
                    for (int e = 0; e < 8; ++e) dr[u][e] = ld64_nt(rd, dofs, (32 * u + e) * 1152 + w * 128);
                if (w < 4) {
#pragma unroll
                    for (int e = 0; e < 8; ++e) gr[4][e] = ld64_nt(rg, go, (128 + e) * 9216);
                    SCHED();
                    frag4(gr[4], gf[4]);
                } else if (w == 5) {
#pragma unroll
                    for (int u = 0; u < 2; ++u)
#pragma unroll
                        for (int e = 0; e < 8; ++e) er[u][e] = ld64_nt(rd, dofs, (32 * u + e) * 1152 + 1024);
                    SCHED();
                    frag4(er[0], ef[0]); frag4(er[1], ef[1]);
                }
                SCHED();
#pragma unroll
                for (int u = 0; u < 4; ++u) frag4(gr[u], gf[u]);
                frag4(dr[0], df[0]); frag4(dr[1], df[1]);
            }
            STAMP(6);
            LAS float* P = (LAS float*)L;
            LAS float* Sx = (LAS float*)(L + 8192);
            float a1g, a1u, a2g, a2u;
            {
                const int ao = (l15 == 0 ? WS_MRB + 2 * 1152 : (l15 == 4 ? WS_MRB + 1152 : OOB)) + (32 * ks0 + 8 * kq) * 2;
                v4u a[5];
                for (int sp = 0;; ++sp) {
                    MEMBAR();
#pragma unroll
                    for (int u = 0; u < 5; ++u) a[u] = ld_sc1(rws, (u < 4 || w < 4) ? ao + 64 * u : OOB);
                    unsigned mx = 0;
#pragma unroll
                    for (int u = 0; u < 5; ++u) mx = umax(umax(mx, a[u].x), a[u].z);
                    if (__all(mx != POISON) || sp > SPIN_MAX) break;
                    __builtin_amdgcn_s_sleep(2);
                }
                v4f acc[4] = {};
#pragma unroll
                for (int u = 0; u < 4; ++u)
#pragma unroll
                    for (int n = 0; n < 4; ++n) acc[n] = mfma16(a[u], gf[u][n], acc[n]);
                if (w < 4) {
#pragma unroll
                    for (int n = 0; n < 4; ++n) acc[n] = mfma16(a[4], gf[4][n], acc[n]);
                }
                const v4f o = {acc[0].x, acc[1].x, acc[2].x, acc[3].x};
                if (lane < 32) *(LAS v4f*)(P + 512 + kq * 512 + w * 64 + l15 * 4) = o;
                BAR();
                LAS float* Q1 = P + 512 + lane; LAS float* Q2 = P + 1024 + lane;
                a1g = (Q1[0] + Q1[64]) + (Q1[256] + Q1[320]); a1u = (Q1[128] + Q1[192]) + (Q1[384] + Q1[448]);
                a2g = (Q2[0] + Q2[64]) + (Q2[256] + Q2[320]); a2u = (Q2[128] + Q2[192]) + (Q2[384] + Q2[448]);
                PIN(a1g); PIN(a1u); PIN(a2g); PIN(a2u);
            }
            STAMP(13);
            const int uo = (l15 < 4 ? WS_U5 + l15 * 1152 : OOB) + (32 * ks0 + 8 * kq) * 2;
            v4f acc[4] = {};
            float rs = 0.f, kg = 0.f, ku = 0.f;
            if (w < 4) {
                v4u a[5];
                for (int sp = 0;; ++sp) {
                    MEMBAR();
#pragma unroll
                    for (int u = 0; u < 5; ++u) a[u] = ld_sc1(rws, uo + 64 * u);
                    unsigned mx = 0;
#pragma unroll
                    for (int u = 0; u < 5; ++u) mx = umax(umax(umax(umax(mx, a[u].x), a[u].y), a[u].z), a[u].w);
                    if (__all(mx != POISON) || sp > SPIN_MAX) break;
                }
                STAMP(7);
#pragma unroll
                for (int u = 0; u < 5; ++u)
#pragma unroll
                    for (int n = 0; n < 4; ++n) acc[n] = mfma16(a[u], gf[u][n], acc[n]);
                SCHED();
                const v4f o = {sum4(acc[0]), sum4(acc[1]), sum4(acc[2]), sum4(acc[3])};
                if (lane < 16) *(LAS v4f*)(P + w * 64 + l15 * 4) = o;
                RAWBAR();
                if (w == 0) {
                    const float mean = sum4(*(LAS v4f*)Sx) * (1.f / M);
                    const float var = rb(fmaxf(sum4(*(LAS v4f*)(Sx + 4)) * (1.f / M) - mean * mean, 0.f));
                    rs = __builtin_amdgcn_rcpf(rb(__builtin_amdgcn_sqrtf(rb(var + 1e-5f))));
                    const float mr = rb(mean) * rs;
                    kg = a2g - mr * a1g; ku = a2u - mr * a1u;
                    PIN(rs); PIN(kg); PIN(ku);
                }
            } else {
                v4u a[4], t[3];
                const int to = WS_T5 + kq * 2304 + ((w - 4) * 2 * 16 + l15) * 16;
                const int to2 = w == 7 ? WS_T5 + kq * 2304 + (128 + l15) * 16 : OOB;
                for (int sp = 0;; ++sp) {
                    MEMBAR();
                    t[0] = ld_sc1(rws, to); t[1] = ld_sc1(rws, to + 256); t[2] = ld_sc1(rws, to2);
#pragma unroll
                    for (int u = 0; u < 4; ++u) a[u] = ld_sc1(rws, uo + 64 * u);
                    unsigned mx = 0;
#pragma unroll
                    for (int u = 0; u < 3; ++u) mx = umax(umax(mx, t[u].x), t[u].z);
#pragma unroll
                    for (int u = 0; u < 4; ++u) mx = umax(umax(umax(umax(mx, a[u].x), a[u].y), a[u].z), a[u].w);
                    if (__all(mx != POISON) || sp > SPIN_MAX) break;
                }
                STAMP(7);
                float s1 = 0.f, s2 = 0.f;
#pragma unroll
                for (int u = 0; u < 3; ++u) {
                    const unsigned t0 = t[u].x, t1 = t[u].y, t2 = t[u].z, t3 = t[u].w;
                    const float xv = red16(red32(u2f(t0), u2f(t2)), red32(u2f(t1), u2f(t3)));
                    s1 += xv; s2 += xv * xv;
                }
                float s = red32(s1, s2); s = red16(s, s); s = row_sum(s);
                if ((lane & 31) == 0) Sx[half * 4 + (w - 4)] = s;
                BAR();
#pragma unroll
                for (int u = 0; u < 4; ++u)
#pragma unroll
                    for (int n = 0; n < 4; ++n) acc[n] = mfma16(a[u], gf[u][n], acc[n]);
                const v4f o = {sum4(acc[0]), sum4(acc[1]), sum4(acc[2]), sum4(acc[3])};
                if (lane < 16) *(LAS v4f*)(P + w * 64 + l15 * 4) = o;
            }
            BAR();
            LAS u16* gs = (LAS u16*)(L + 8448);
            if (w == 0) {
                LAS float* Q = P + lane;
                const float G = (Q[0] + Q[64]) + (Q[256] + Q[320]), U = (Q[128] + Q[192]) + (Q[384] + Q[448]);
                const unsigned gu = pkbf(rs * G + kg, rs * U + ku);
                const float ag = bflo(gu), au = bfhi(gu);
                constexpr float K0 = -2.f * 1.4426950408889634f * 0.7978845608028654f, K1 = K0 * 0.044715f;
                const float e = __builtin_amdgcn_exp2f(ag * (K0 + K1 * ag * ag));
                const float ge = rb(ag * __builtin_amdgcn_rcpf(1.f + e));
                gs[lane] = (u16)pkbf(ge * au, 0.f);
            }
            BAR();
            STAMP(14);
            {
                const v4u g0 = *(LAS v4u*)(gs + 8 * kq), g1 = *(LAS v4u*)(gs + 32 + 8 * kq);
                v4f y[4] = {};
#pragma unroll
                for (int n = 0; n < 4; ++n) y[n] = mfma16(g0, df[0][n], y[n]);
#pragma unroll
                for (int n = 0; n < 4; ++n) y[n] = mfma16(g1, df[1][n], y[n]);
                const v4u o = {f2u(y[0].x), f2u(y[1].x), f2u(y[2].x), f2u(y[3].x)};
                if (lane < 16) st_sc1(o, rws, WS_H6 + f * 2304 + w * 256 + l15 * 16);
                if (w == 5) {
                    v4f z[4] = {};
#pragma unroll
                    for (int n = 0; n < 4; ++n) z[n] = mfma16(g0, ef[0][n], z[n]);
#pragma unroll
                    for (int n = 0; n < 4; ++n) z[n] = mfma16(g1, ef[1][n], z[n]);
                    const v4u o2 = {f2u(z[0].x), f2u(z[1].x), f2u(z[2].x), f2u(z[3].x)};
                    if (lane < 16) st_sc1(o2, rws, WS_H6 + f * 2304 + 2048 + l15 * 16);
                }
            }
            STAMP(8);
        } else if (b >= 20) {
            const int o6 = b - 20, kg = o6 / 9, nt = o6 - 9 * kg;
            const int head = 8 * kg + w;
            v4u of[4][4];
            {
                const rsrc_t ro = RSRC(wo, 4096 * M * 2);
                const int oo = (128 * head + 8 * kq) * 1152 + nt * 128 + l15 * 8;
                v2u orr[4][8];
#pragma unroll
                for (int u = 0; u < 4; ++u)
#pragma unroll
                    for (int e = 0; e < 8; ++e) orr[u][e] = ld64_nt(ro, oo, (32 * u + e) * 1152);
                SCHED();
#pragma unroll
                for (int u = 0; u < 4; ++u) frag4(orr[u], of[u]);
            }
            STAMP(6);
            float ga0 = 0.f, ga1 = 0.f, c10 = 0.f, c11 = 0.f, x0 = 0.f, x1 = 0.f;
            if (w == 0) {
                const int mo = (64 * nt + 2 * (lane & 31)) * 2;
                const unsigned xr = __builtin_amdgcn_raw_buffer_load_b32(rx, kg == 0 ? mo : OOB, 0, 0);
                unsigned g, c;
                for (int sp = 0;; ++sp) {
                    MEMBAR();
                    g = ld32_sc1(rws, WS_MRB + mo); c = ld32_sc1(rws, WS_MRB + 2 * 1152 + mo);
                    if (__all(g != POISON && c != POISON) || sp > SPIN_MAX) break;
                    __builtin_amdgcn_s_sleep(2);
                }
                ga0 = bflo(g); ga1 = bfhi(g); c10 = bflo(c); c11 = bfhi(c); x0 = bflo(xr); x1 = bfhi(xr);
                PIN(ga0); PIN(ga1); PIN(c10); PIN(c11); PIN(x0); PIN(x1);
            }
            STAMP(13);
            const int co = (l15 < 8 ? WS_CTX + ((head >> 1) * 8 + l15) * 512 + (head & 1) * 256 : OOB) + kq * 16;
            v4u a[4];
            for (int sp = 0;; ++sp) {
                MEMBAR();
#pragma unroll
                for (int u = 0; u < 4; ++u) a[u] = ld_sc1(rws, co + 64 * u);
                unsigned mx = 0;
#pragma unroll
                for (int u = 0; u < 4; ++u) mx = umax(umax(mx, a[u].x), a[u].z);
                if (__all(mx != POISON) || sp > SPIN_MAX) break;
            }
            STAMP(7);
            v4f acc[4] = {};
#pragma unroll
            for (int u = 0; u < 4; ++u)
#pragma unroll
                for (int n = 0; n < 4; ++n) acc[n] = mfma16(a[u], of[u][n], acc[n]);
            v2f c; c.x = red16(sum4(acc[0]), sum4(acc[2])); c.y = red16(sum4(acc[1]), sum4(acc[3]));
            if (lane < 32) *(LAS v2f*)(red + w * 64 + l15 * 4 + kq * 2) = c;
            BAR();
            if (tid < 32) {
                LAS v2f* R = (LAS v2f*)red + lane;
                const v2f s = ((R[0] + R[32]) + (R[64] + R[96])) + ((R[128] + R[160]) + (R[192] + R[224]));
                const float t0 = x0 + ga0 * s.x, t1 = x1 + ga1 * s.y;
                st32_sc1(pkbf(t0 * c10, t1 * c11), rws, WS_U5 + kg * 1152 + nt * 128 + lane * 4);
                st64_sc1(v2u{f2u(t0), f2u(t1)}, rws, WS_T5 + kg * 2304 + nt * 256 + lane * 8);
            }
            STAMP(8);
        } else if (b < 18) {
            {
                v4u mw[18]; unsigned mc[18];
#pragma unroll
                for (int i = 0; i < 18; ++i) mc[i] = __builtin_amdgcn_raw_buffer_load_b16(rc, (72 * w + 4 * i + kq) * 2, 0, 0);
#pragma unroll
                for (int i = 0; i < 18; ++i) mw[i] = ld_nt(rmod, (72 * w + 4 * i + kq) * 6912 + 2304 + b * 256 + l15 * 16);
                SCHED();
#pragma unroll
                for (int i = 0; i < 18; ++i) { mc[i] = f2u(silu_bf(bflo(mc[i]))); PIN(mc[i]); }
                ZERO8(acc);
#pragma unroll
                for (int i = 0; i < 18; ++i) dot8z(acc, mw[i], mc[i] >> 16, mc[i]);
                const float b0 = red32(acc[0], acc[4]), b1 = red32(acc[1], acc[5]), b2 = red32(acc[2], acc[6]), b3 = red32(acc[3], acc[7]);
                v2f c; c.x = red16(b0, b2); c.y = red16(b1, b3);
                *(LAS v2f*)(red + w * 128 + l15 * 8 + kq * 2) = c;
                BAR();
                if (tid < 32) {
                    v4f s = *(LAS v4f*)(red + tid * 4);
#pragma unroll
                    for (int k = 1; k < 8; ++k) s += *(LAS v4f*)(red + k * 128 + tid * 4);
                    const int col = 128 * b + 4 * tid;
                    const float one = (col >= 1152 && col < 1728) ? 1.f : 0.f;
                    st64_sc1(v2u{pkbf(rb(s.x) + one, rb(s.y) + one), pkbf(rb(s.z) + one, rb(s.w) + one)}, rws, WS_MRB + col * 2);
                }
                STAMP(6);
            }
            if (w == 0) {
                const int cg = 8 * b + (lane & 7);
                const int m = 4 * cg + 2 * half + (kq & 1);
                v4u t; unsigned gb;
                for (int sp = 0;; ++sp) {
                    MEMBAR();
                    gb = ld32_sc1(rws, WS_MRB + 3 * 1152 + (m & ~1) * 2);
                    t = ld_sc1(rws, WS_T5 + kq * 2304 + cg * 16);
                    if (__all(umax(umax(t.x, t.z), gb) != POISON) || sp > SPIN_MAX) break;
                    __builtin_amdgcn_s_sleep(4);
                }
                const unsigned t0 = t.x, t1 = t.y, t2 = t.z, t3 = t.w;
                float x2 = rb(red16(red32(u2f(t0), u2f(t2)), red32(u2f(t1), u2f(t3))));
                float gsel = (kq & 1) ? bfhi(gb) : bflo(gb);
                PIN(x2); PIN(gsel);
                STAMP(7);
                v4f s;
                for (int sp = 0;; ++sp) {
                    MEMBAR();
                    v4u y[9];
#pragma unroll
                    for (int i = 0; i < 9; ++i) y[i] = ld_sc1(rws, WS_H6 + (8 * i + (lane >> 3)) * 2304 + cg * 16);
                    SCHED();
                    s = __builtin_bit_cast(v4f, y[0]);
#pragma unroll
                    for (int i = 1; i < 9; ++i) s += __builtin_bit_cast(v4f, y[i]);
                    if (__all(s.x == s.x) || sp > SPIN_MAX) break;
                }
                const float yv = rb(add_x8(red16(red32(s.x, s.z), red32(s.y, s.w))));
                const float o = x2 + rb(gsel * yv);
                if ((lane & 8) == 0) out[m] = (u16)pkbf(o, o);
                if (b == 0 && lane == 0) st32_sc1(tag, rws, WS_EPOCH);
                STAMP(8);
            }
        }
    } else {
        const int a = bid - 128;
        constexpr int QOFF = 131072;
        unsigned tag = ld32_sc1(rws, WS_EPOCH);
        {
            const v4u poison = {POISON, POISON, POISON, POISON};
            if (a < 9) {
#pragma unroll
                for (int i = 0; i < 4; ++i) st_sc1(poison, rws, WS_QA + a * 32768 + (i * NT + tid) * 16);
                if (a == 0) { DRAIN(); BAR(); if (tid == 0) st32_sc1(tag + 1u, rws, WS_F1); }
            }
#pragma unroll
            for (int i = 0; i < 4; ++i) st_sc1(poison, rws, WS_INB + a * 32768 + (i * NT + tid) * 16);
            if (w == 7) {
                const rsrc_t rcode = RSRC((const void*)(pc0 & ~255ull), CODE_BYTES);
                unsigned acc = 0;
#pragma unroll
                for (int i = 0; i < (CODE_BYTES + 8191) / 8192; ++i) acc ^= __builtin_amdgcn_raw_buffer_load_b32(rcode, (i * 64 + lane) * 128, 0, 0);
                asm volatile("" :: "v"(acc));
            }
            STAMP(9);
            DRAIN();
            STAMP(10);
            BAR();
            PIN(tag); tag += 1u;
            if (tid == 0) st32_sc1(tag, rws, WS_CL + a * 128);
            if (w == 0) {
                for (int sp = 0;; ++sp) {
                    MEMBAR();
                    const unsigned f = ld32_sc1(rws, WS_F1);
                    if (__all(f == tag) || sp > SPIN_MAX) break;
                    __builtin_amdgcn_s_sleep(2);
                }
                if (A_DELAY) { while (rt() - t_start < (u64)A_DELAY) __builtin_amdgcn_s_sleep(1); }
            }
            BAR();
        }
        STAMP(1);
        {
            const rsrc_t rkv = RSRC(ckv, SK * DC * 2);
            const int g0 = ((w & 3) << 2) | ((w >> 2) & 3), g1 = ((w & 3) << 2) | ((2 + (w >> 2)) & 3);
            const int vo0 = (lane ^ g0) * 16, vo1 = (lane ^ g1) * 16;
#pragma unroll
            for (int i = 0; i < 16; ++i)
                __builtin_amdgcn_raw_ptr_buffer_load_lds(rkv, (LAS void*)(L + (8 * i + w) * 1024), 16, (i & 1) ? vo1 : vo0, (a * 128 + 8 * i + w) * 1024, 0, AUX_NT);
            SCHED();
        }
        STAMP(2);
        const int hpair = a >> 3, cq = a & 7;
        unsigned ad[8];
        if (w < 4) {
            const int r = lane & 31, g = ((r & 3) << 2) | ((r >> 2) & 3);
#pragma unroll
            for (int j = 0; j < 8; ++j) { ad[j] = w * 32768 + r * 1024 + (((2 * j + half) ^ g) * 16); PIN(ad[j]); }
        } else {
            const int blk = kq & 1, q = l15 >> 2, p = lane & 3;
#pragma unroll
            for (int j = 0; j < 8; ++j) {
                const int t = j >> 1, tt = j & 1, g = (q << 2) | ((2 * tt + half) & 3);
                ad[j] = (8 * tt + 4 * half + q) * 1024 + (w - 4) * 256 + (((4 * t + 2 * blk + (p >> 1)) ^ g) * 16) + 8 * (p & 1); PIN(ad[j]);
            }
        }
        unsigned qw[4];
#pragma unroll
        for (int i = 0; i < 4; ++i) { qw[i] = QOFF + (4 * w + i) * 1024 + ((lane ^ ((i << 2) | (w & 3))) * 16); PIN(qw[i]); }
        asm volatile("s_waitcnt vmcnt(%0)" :: "i"(QWAIT) : "memory");
        STAMP(11);
        {
            v4u q[4];
            for (int sp = 0;; ++sp) {
                MEMBAR();
#pragma unroll
                for (int i = 0; i < 4; ++i) q[i] = ld_sc1(rws, WS_QA + (4 * w + i) * 1024 + lane * 16);
                if (__all(umax(umax(q[0].x, q[1].x), umax(q[2].x, q[3].x)) != POISON) || sp > SPIN_MAX) break;
            }
            DRAIN();
            STAMP(12);
#pragma unroll
            for (int i = 0; i < 4; ++i) *(LAS v4u*)(L + qw[i]) = q[i];
        }
        LAS float* mx = (LAS float*)(L + QOFF + 8192);
        LAS float* ls = (LAS float*)(L + QOFF + 8704);
        if (w < 4) {
            v2u vr[2][8];
            {
                const rsrc_t rv = RSRC(wuv, DC * 8192);
                const int vo = (128 * (cq >> 1) + 16 * (cq & 1) + 2 * kq) * 8192 + hpair * 512 + w * 128 + l15 * 8;
#pragma unroll
                for (int u = 0; u < 2; ++u)
#pragma unroll
                    for (int e = 0; e < 8; ++e) vr[u][e] = ld64_nt(rv, vo, (8 * u + (e >> 2) + 32 * (e & 3)) * 8192);
                SCHED();
            }
            BAR();
            STAMP(3); CYC0();
            const int qd = QOFF - w * 32768;
            constexpr int PD = 6;
            v4u kf[32], qf[32];
            v16f acc = {};
#pragma unroll
            for (int s = 0; s < PD; ++s) { kf[s] = *(LAS v4u*)(L + ad[s & 7] + (s >> 3) * 256); qf[s] = *(LAS v4u*)(L + (ad[s & 7] + qd) + (s >> 3) * 256); }
            SCHED();
#pragma unroll
            for (int s = 0; s < 32; ++s) {
                acc = mfma32(kf[s], qf[s], acc);
                if (s + PD < 32) { kf[s + PD] = *(LAS v4u*)(L + ad[(s + PD) & 7] + ((s + PD) >> 3) * 256); qf[s + PD] = *(LAS v4u*)(L + (ad[(s + PD) & 7] + qd) + ((s + PD) >> 3) * 256); }
                SCHED();
            }
            RAWBAR();
            STAMP(13); CYC1();
            float m = -3.0e38f;
#pragma unroll
            for (int i = 0; i < 16; ++i) m = fmaxf(m, acc[i]);
            m = __builtin_ceilf(max32(m, m));
            float p[16];
#pragma unroll
            for (int i = 0; i < 16; ++i) p[i] = __builtin_amdgcn_exp2f(acc[i] - m);
            const v4u f0 = {pkbf(p[0], p[1]), pkbf(p[2], p[3]), pkbf(p[4], p[5]), pkbf(p[6], p[7])};
            const v4u f1 = {pkbf(p[8], p[9]), pkbf(p[10], p[11]), pkbf(p[12], p[13]), pkbf(p[14], p[15])};
            *(LAS v4u*)(L + QOFF + (2 * w) * 1024 + lane * 16) = f0;
            *(LAS v4u*)(L + QOFF + (2 * w + 1) * 1024 + lane * 16) = f1;
            mx[w * 32 + (lane & 31)] = m;
            BAR();
            STAMP(4);
            float l = 0.f;
#pragma unroll
            for (int i = 0; i < 16; ++i) l += p[i];
            l = red32(l, l);
            ls[w * 32 + (lane & 31)] = l;
            BAR();
            if (w == 1 && lane < 16) {
                v2f mk[4], lk[4];
#pragma unroll
                for (int k = 0; k < 4; ++k) { mk[k] = *(LAS v2f*)(mx + 32 * k + 2 * lane); lk[k] = *(LAS v2f*)(ls + 32 * k + 2 * lane); }
                const float m0 = fmaxf(fmaxf(mk[0].x, mk[1].x), fmaxf(mk[2].x, mk[3].x)), m1 = fmaxf(fmaxf(mk[0].y, mk[1].y), fmaxf(mk[2].y, mk[3].y));
                float l0 = 0.f, l1 = 0.f;
#pragma unroll
                for (int k = 0; k < 4; ++k) { l0 += lk[k].x * __builtin_amdgcn_exp2f(mk[k].x - m0); l1 += lk[k].y * __builtin_amdgcn_exp2f(mk[k].y - m1); }
                st_sc1(pack4(m0, m1, l0, l1, tag), rws, WS_ST + a * 256 + lane * 16);
            }
            v4u vf[2][4];
            frag4(vr[0], vf[0]); frag4(vr[1], vf[1]);
            LAS u16* wt = (LAS u16*)(L + QOFF + 12288);
            if (w == 0) {
                v4u s0, s1;
                for (int sp = 0;; ++sp) {
                    MEMBAR();
                    s0 = ld_sc1(rws, WS_ST + lane * 256 + hpair * 16); s1 = ld_sc1(rws, WS_ST + (64 + lane) * 256 + hpair * 16);
                    if (__all(s0.w == tag && s1.w == tag) || sp > SPIN_MAX) break;
                    __builtin_amdgcn_s_sleep(1);
                }
                STAMP(6);
                const v4f sa = unpack4(s0), sb = unpack4(s1);
                float mm = max32(fmaxf(sa.x, sb.x), fmaxf(sa.y, sb.y));
                mm = row_max(max16(mm));
                auto sw = __builtin_amdgcn_permlane32_swap(f2u(mm), f2u(mm), false, false);
                const unsigned M0 = sw[0], M1 = sw[1];
                const float e00 = rb(__builtin_amdgcn_exp2f(sa.x - u2f(M0))), e01 = rb(__builtin_amdgcn_exp2f(sa.y - u2f(M1)));
                const float e10 = rb(__builtin_amdgcn_exp2f(sb.x - u2f(M0))), e11 = rb(__builtin_amdgcn_exp2f(sb.y - u2f(M1)));
                float ll = red32(e00 * sa.z + e10 * sb.z, e01 * sa.w + e11 * sb.w);
                ll = red16(ll, ll); ll = __builtin_amdgcn_rcpf(row_sum(ll));
                ((LAS float*)(L + QOFF + 12800))[half] = ll;
                wt[lane] = (u16)(f2u(e00) >> 16); wt[64 + lane] = (u16)(f2u(e10) >> 16); wt[128 + lane] = (u16)(f2u(e01) >> 16); wt[192 + lane] = (u16)(f2u(e11) >> 16);
            }
            const int io = WS_INB + a * 32768 + (32 * w + 8 * kq) * 256 + l15 * 16;
            BAR();
            v4u wa = *(LAS v4u*)(wt + (lane & 1) * 128 + 32 * w + 8 * kq);
            float rl = ((LAS float*)(L + QOFF + 12800))[w >> 1];
            STAMP(14);
            v4u d[8];
            for (int sp = 0;; ++sp) {
                MEMBAR();
#pragma unroll
                for (int e = 0; e < 8; ++e) d[e] = ld_sc1(rws, io + e * 256);
                const unsigned mxp = umax(umax(umax(d[0].x, d[1].x), umax(d[2].x, d[3].x)), umax(umax(d[4].x, d[5].x), umax(d[6].x, d[7].x)));
                if (__all(mxp != POISON) || sp > SPIN_MAX) break;
            }
            STAMP(7);
            float lat[2][4];
#pragma unroll
            for (int t = 0; t < 4; ++t) {
                v4u lo, hi;
#pragma unroll
                for (int p2 = 0; p2 < 4; ++p2) { const unsigned u0 = d[2 * p2][t], u1 = d[2 * p2 + 1][t]; lo[p2] = perm(u1, u0, 0x05040100u); hi[p2] = perm(u1, u0, 0x07060302u); }
                const v4f z = {0.f, 0.f, 0.f, 0.f};
                lat[0][t] = mfma16(wa, lo, z).x; lat[1][t] = mfma16(wa, hi, z).y;
            }
            LAS unsigned char* la = L + QOFF + 13312;
            {
                const v2u o0 = {pkbf(lat[0][0], lat[0][1]), pkbf(lat[0][2], lat[0][3])}, o1 = {pkbf(lat[1][0], lat[1][1]), pkbf(lat[1][2], lat[1][3])};
                if (lane < 16) { *(LAS v2u*)(la + w * 128 + l15 * 8) = o0; *(LAS v2u*)(la + 512 + w * 128 + l15 * 8) = o1; }
            }
            BAR();
            {
                LAS unsigned char* lr = la + (w >> 1) * 512 + (lane & 3) * 128 + kq * 16;
                const v4u a0 = *(LAS v4u*)lr, a1 = *(LAS v4u*)(lr + 64);
                v4f y[4] = {};
#pragma unroll
                for (int n = 0; n < 4; ++n) y[n] = mfma16(a0, vf[0][n], y[n]);
#pragma unroll
                for (int n = 0; n < 4; ++n) y[n] = mfma16(a1, vf[1][n], y[n]);
                const v2u o = {pkbf(sum4(y[0]) * rl, sum4(y[1]) * rl), pkbf(sum4(y[2]) * rl, sum4(y[3]) * rl)};
                if (lane < 16) st64_sc1(o, rws, WS_CTX + a * 512 + w * 128 + l15 * 8);
            }
            STAMP(8);
        } else {
            BAR();
            const int v = w - 4;
            const int col = lane & 31;
            int ob = WS_INB + ((2 * half) * 8 + 2 * v + (col >> 4)) * 32768 + a * 256 + (col & 15) * 16;
            PIN(ob);
            RAWBAR();
            v4s vb[4][16];
#define VREAD(t) _Pragma("unroll") for (int k = 0; k < 16; ++k) vb[t][k] = __builtin_amdgcn_ds_read_tr16_b64_v4i16((LAS v4s*)(L + (ad[2 * (t) + (k & 1)] + (k >> 3) * 65536) + ((k >> 1) & 3) * 16384))
            VREAD(0); VREAD(1);
            BAR();
            STAMP(4); CYC0();
            v4u pa[8];
            const float m0 = mx[col], m1 = mx[32 + col], m2 = mx[64 + col], m3 = mx[96 + col];
#pragma unroll
            for (int ks = 0; ks < 8; ++ks) pa[ks] = *(LAS v4u*)(L + QOFF + ks * 1024 + lane * 16);
            SCHED();
            const float mm = fmaxf(fmaxf(fmaxf(m0, m1), m2), m3);
            const float mk[4] = {m0, m1, m2, m3};
            unsigned pk[8][4];
            v16f acc[4];
#pragma unroll
            for (int t = 0; t < 4; ++t) {
#pragma unroll
                for (int ks = 0; ks < 8; ++ks) {
                    if (t == 0) {
                        const unsigned sub = (unsigned)fminf(mm - mk[ks >> 1], 255.f) * 0x00800080u;
#pragma unroll
                        for (int i = 0; i < 4; ++i) { const unsigned u = pa[ks][i]; pa[ks][i] = __builtin_bit_cast(unsigned, __builtin_elementwise_sub_sat(__builtin_bit_cast(v2us, u), __builtin_bit_cast(v2us, sub))); }
                    }
                    const v4u bv = __builtin_bit_cast(v4u, __builtin_shufflevector(vb[t][2 * ks], vb[t][2 * ks + 1], 0, 1, 2, 3, 4, 5, 6, 7));
                    const v16f z = {};
                    acc[t] = mfma32(pa[ks], bv, ks ? acc[t] : z);
                    if (t < 2) {
                        vb[t + 2][2 * ks] = __builtin_amdgcn_ds_read_tr16_b64_v4i16((LAS v4s*)(L + (ad[2 * (t + 2)] + (ks >> 2) * 65536) + (ks & 3) * 16384));
                        vb[t + 2][2 * ks + 1] = __builtin_amdgcn_ds_read_tr16_b64_v4i16((LAS v4s*)(L + (ad[2 * (t + 2) + 1] + (ks >> 2) * 65536) + (ks & 3) * 16384));
                    }
                    if (t > 0 && ks >= 2 && ks < 6) {
                        pk[2 * (ks - 2)][t - 1] = pkbf(acc[t - 1][4 * (ks - 2)], acc[t - 1][4 * (ks - 2) + 1]);
                        pk[2 * (ks - 2) + 1][t - 1] = pkbf(acc[t - 1][4 * (ks - 2) + 2], acc[t - 1][4 * (ks - 2) + 3]);
                    }
                    if (t == 0 && ks == 5) RAWBAR();
                    SCHED();
                }
            }
            STAMP(13); CYC1();
#pragma unroll
            for (int e = 0; e < 8; ++e) {
                const v4u o = {pk[e][0], pk[e][1], pk[e][2], pkbf(acc[3][2 * e], acc[3][2 * e + 1])};
                __builtin_amdgcn_raw_buffer_store_b128(o, rws, ob, (4 * (e >> 1) + (e & 1)) * 8 * 32768, AUX_SC1);
            }
            STAMP(5);
        }
    }
    STAMP_FLUSH();
}

extern "C" void kernel_launch(void* const* d_in, const int* in_sizes, int n_in, void* d_out, int out_size, void* d_ws, size_t ws_size, hipStream_t stream) {
    fused_kernel<<<256, NT, 0, stream>>>((const u16*)d_in[0], (const u16*)d_in[1], (const u16*)d_in[2], (const u16*)d_in[3], (const u16*)d_in[4], (const u16*)d_in[5],
                                         (const u16*)d_in[7], (const u16*)d_in[8], (const u16*)d_in[9], (const u16*)d_in[10], (const u16*)d_in[11], (u16*)d_out, (unsigned char*)d_ws);
    k_last<<<1, 64, 0, stream>>>((unsigned char*)d_ws);
}
```

```cpp
#include <hip/hip_runtime.h>
#include <hip/hip_bf16.h>

#define STAMPS 0
#define LATE_DELAY 500
#define A_DELAY 0
#define B_DELAY 0
#define CL_DELAY 0
#define K3E 8
#define DMAW 4
#define AUXF 2
#define AUXL 2
#define SLOW_EARLY 1
#define CLB 1
#define S1DUP 0
#define KPF 1
#define KPF_DELAY 0
#define AUXK 2
#define K3LATE 1
#define QHINT 0
#define AUXS 2
#define WQE 5
#define K3A 0
#define DMAW0 DMAW
#define DMAN0 4
#define UVA 10
#define S5DRAIN 1
#define DPF_SLEEP 8
#define UKG 12
#define CH0 80
#define CHW 4
#define CH_DELAY 200
#define PFW 3
#define LATE_F 450
#define FW0 36
#define FN0 0
#define KVMAP 2
#define KVROT 23
#define HINTI 13
#define MOD_EARLY 0
#define CUE5 0
#define CUE6 80
#define CUE7 40
#define CUE8 50
#define QWAIT 2

#define WS_BASE (248u  __H1__ )
typedef unsigned short u16;
typedef unsigned long long u64;
typedef unsigned v4u __attribute__((ext_vector_type(4)));
typedef unsigned v2u __attribute__((ext_vector_type(2)));
typedef float v4f __attribute__((ext_vector_type(4)));
typedef float v2f __attribute__((ext_vector_type(2)));
typedef float v16f __attribute__((ext_vector_type(16)));
typedef short v4s __attribute__((ext_vector_type(4)));
typedef unsigned short v2us __attribute__((ext_vector_type(2)));
typedef __bf16 v2bf __attribute__((ext_vector_type(2)));
typedef __bf16 v8bf __attribute__((ext_vector_type(8)));
#define LAS __attribute__((address_space(3)))
#define GAS __attribute__((address_space(1)))
typedef __amdgpu_buffer_rsrc_t rsrc_t;

constexpr int M = 576, DC = 512, SK = 16384, FF = 4608;
constexpr int NT = 512;
constexpr int QOFF = 131072;
constexpr int CODE_BYTES = 28416;
constexpr int SPIN_MAX = 1 << 16;
constexpr unsigned POISON = 0xFFFFFFFFu;
constexpr int OOB = 0x40000000;
constexpr int WS_EPOCH = 0;
constexpr int WS_CL = 4096;
constexpr int WS_H1 = WS_CL + 128 * 128;
constexpr int WS_ST = WS_H1 + 3 * 288 * 16;
constexpr int WS_QA = 256 * 1024;
constexpr int WS_PZ = WS_QA + 32768;
constexpr int WS_CTX = WS_PZ;
constexpr int WS_U5 = WS_CTX + 65536;
constexpr int WS_T5 = WS_U5 + 4608;
constexpr int WS_MRB = WS_T5 + 9216;
constexpr int WS_H6 = WS_MRB + 4608;
constexpr int WS_CK = 768 * 1024;
constexpr int WS_INB = 1 << 20;
constexpr int WS_STAMP = WS_INB + (4 << 20);
constexpr int WS_BYTES = WS_STAMP + 256 * 8 * 16 * 4;
static_assert(WS_ST + 32768 <= WS_QA && WS_H6 + 72 * 2304 <= WS_PZ + 8 * 32768 && WS_PZ + 8 * 32768 <= WS_CK && WS_CK + 131072 <= WS_INB, "ws map");
static_assert(WS_H1 % 128 == 0 && WS_ST % 128 == 0 && WS_U5 % 128 == 0 && WS_T5 % 128 == 0 && WS_MRB % 128 == 0 && WS_H6 % 128 == 0, "line aligned");

#define RSRC(p, bytes) __builtin_amdgcn_make_buffer_rsrc((void*)(p), 0, (bytes), 0x00020000)
#define AUX_NT 2
#define AUX_SC1 16
__device__ __forceinline__ v4u ld_nt(rsrc_t r, int off) { return __builtin_amdgcn_raw_buffer_load_b128(r, off, 0, AUXL); }
__device__ __forceinline__ v2u ld64_nt(rsrc_t r, int off, int so) { return __builtin_amdgcn_raw_buffer_load_b64(r, off, so, AUXL); }
__device__ __forceinline__ v4u ld_sc1(rsrc_t r, int off) { return __builtin_amdgcn_raw_buffer_load_b128(r, off, 0, AUX_SC1); }
__device__ __forceinline__ unsigned ld32_sc1(rsrc_t r, int off) { return __builtin_amdgcn_raw_buffer_load_b32(r, off, 0, AUX_SC1); }
__device__ __forceinline__ void st_sc1(v4u v, rsrc_t r, int off) { __builtin_amdgcn_raw_buffer_store_b128(v, r, off, 0, AUX_SC1); }
__device__ __forceinline__ void st64_sc1(v2u v, rsrc_t r, int off) { __builtin_amdgcn_raw_buffer_store_b64(v, r, off, 0, AUX_SC1); }
__device__ __forceinline__ void st32_sc1(unsigned v, rsrc_t r, int off) { __builtin_amdgcn_raw_buffer_store_b32(v, r, off, 0, AUX_SC1); }
__device__ __forceinline__ void dma16(rsrc_t r, int lds, int voff, int soff) {
    asm volatile("s_mov_b32 m0, %0\n\ts_nop 0\n\tbuffer_load_dwordx4 %1, %2, %3 offen nt lds" :: "s"(lds), "v"(voff), "s"(r), "s"(soff) : "memory");
}
#define MEMBAR() asm volatile("" ::: "memory")
#define DRAIN() asm volatile("s_waitcnt vmcnt(0)" ::: "memory")
#define SCHED() __builtin_amdgcn_sched_barrier(0)
#define PIN(x) asm volatile("" : "+v"(x))
#define LGKM0() do { MEMBAR(); __builtin_amdgcn_s_waitcnt(0xC07F); MEMBAR(); } while (0)
#define BAR() do { LGKM0(); asm volatile("s_barrier" ::: "memory"); } while (0)
#define RAWBAR() do { asm volatile("s_barrier" ::: "memory"); } while (0)

__device__ __forceinline__ float u2f(unsigned u) { return __builtin_bit_cast(float, u); }
__device__ __forceinline__ unsigned f2u(float f) { return __builtin_bit_cast(unsigned, f); }
__device__ __forceinline__ float bflo(unsigned u) { return u2f(u << 16); }
__device__ __forceinline__ float bfhi(unsigned u) { return u2f(u & 0xFFFF0000u); }
__device__ __forceinline__ unsigned pkbf(float lo, float hi) { v2f p = {lo, hi}; v2bf q = __builtin_convertvector(p, v2bf); return __builtin_bit_cast(unsigned, q); }
__device__ __forceinline__ float rb(float f) { return bflo(pkbf(f, f)); }
__device__ __forceinline__ float dot2(unsigned a, unsigned b, float c) { return __builtin_amdgcn_fdot2_f32_bf16(__builtin_bit_cast(v2bf, a), __builtin_bit_cast(v2bf, b), c, false); }
__device__ __forceinline__ unsigned perm(unsigned hi, unsigned lo, unsigned sel) { return __builtin_amdgcn_perm(hi, lo, sel); }
__device__ __forceinline__ unsigned umax(unsigned a, unsigned b) { return a > b ? a : b; }
__device__ __forceinline__ v4f mfma16(v4u a, v4u b, v4f c) { return __builtin_amdgcn_mfma_f32_16x16x32_bf16(__builtin_bit_cast(v8bf, a), __builtin_bit_cast(v8bf, b), c, 0, 0, 0); }
__device__ __forceinline__ v16f mfma32(v4u a, v4u b, v16f c) { return __builtin_amdgcn_mfma_f32_32x32x16_bf16(__builtin_bit_cast(v8bf, a), __builtin_bit_cast(v8bf, b), c, 0, 0, 0); }
__device__ __forceinline__ float sum4(v4f a) { return (a.x + a.z) + (a.y + a.w); }
__device__ __forceinline__ v4u pack4(float a, float b, float c, float d, unsigned tag) {
    const unsigned ud = f2u(d);
    v4u g; g.x = perm(f2u(a), ud, 0x07060501u); g.y = perm(f2u(b), ud, 0x07060502u); g.z = perm(f2u(c), ud, 0x07060503u); g.w = tag; return g;
}
__device__ __forceinline__ v4f unpack4(v4u g) {
    const unsigned gx = g.x, gy = g.y, gz = g.z;
    const unsigned t = perm(gy, gx, 0x0c04000cu);
    const unsigned d = perm(gz, t, 0x04020100u);
    v4f r = {u2f(gx & 0xFFFFFF00u), u2f(gy & 0xFFFFFF00u), u2f(gz & 0xFFFFFF00u), u2f(d)}; return r;
}
__device__ __forceinline__ v4f unpack4n(v4u g) {
    const unsigned gx = g.x, gy = g.y, gz = g.z;
    v4f r = {u2f(gx), u2f(gy), u2f(gz), u2f(perm(gz, perm(gy, gx, 0x0c04000cu), 0x04020100u))}; return r;
}
__device__ __forceinline__ float red32(float a, float b) { auto r = __builtin_amdgcn_permlane32_swap(f2u(a), f2u(b), false, false); const unsigned x = r[0], y = r[1]; return u2f(x) + u2f(y); }
__device__ __forceinline__ float red16(float a, float b) { auto r = __builtin_amdgcn_permlane16_swap(f2u(a), f2u(b), false, false); const unsigned x = r[0], y = r[1]; return u2f(x) + u2f(y); }
__device__ __forceinline__ float max32(float a, float b) { auto r = __builtin_amdgcn_permlane32_swap(f2u(a), f2u(b), false, false); const unsigned x = r[0], y = r[1]; return fmaxf(u2f(x), u2f(y)); }
__device__ __forceinline__ float max16(float a) { auto r = __builtin_amdgcn_permlane16_swap(f2u(a), f2u(a), false, false); const unsigned x = r[0], y = r[1]; return fmaxf(u2f(x), u2f(y)); }
template <int CTRL> __device__ __forceinline__ float dpp(float v) { return u2f((unsigned)__builtin_amdgcn_update_dpp(0, (int)f2u(v), CTRL, 0xf, 0xf, true)); }
__device__ __forceinline__ float add_x8(float v) { return v + dpp<0x128>(v); }
__device__ __forceinline__ float add_r4(float v) { return v + dpp<0x124>(v); }
__device__ __forceinline__ float add_x2(float v) { return v + dpp<0x4E>(v); }
__device__ __forceinline__ float add_x1(float v) { return v + dpp<0xB1>(v); }
__device__ __forceinline__ float row_sum(float v) { return add_r4(add_x8(add_x2(add_x1(v)))); }
__device__ __forceinline__ float row_max(float v) { v = fmaxf(v, dpp<0xB1>(v)); v = fmaxf(v, dpp<0x4E>(v)); v = fmaxf(v, dpp<0x128>(v)); return fmaxf(v, dpp<0x124>(v)); }
template <int CTRL> __device__ __forceinline__ unsigned addu_dpp(unsigned v) { return v + (unsigned)__builtin_amdgcn_update_dpp(0, (int)v, CTRL, 0xf, 0xf, true); }
__device__ __forceinline__ unsigned redu16(unsigned a, unsigned b) { auto r = __builtin_amdgcn_permlane16_swap(a, b, false, false); const unsigned x = r[0], y = r[1]; return x + y; }
__device__ __forceinline__ unsigned redu32(unsigned a, unsigned b) { auto r = __builtin_amdgcn_permlane32_swap(a, b, false, false); const unsigned x = r[0], y = r[1]; return x + y; }
__device__ __forceinline__ float wave_sum(float v) { v = row_sum(v); v = red16(v, v); return red32(v, v); }
__device__ __forceinline__ void interleave(v4u& a, v4u& b) {
    const unsigned a0 = a.x, a1 = a.y, a2 = a.z, a3 = a.w, b0 = b.x, b1 = b.y, b2 = b.z, b3 = b.w;
    a.x = perm(b0, a0, 0x05040100u); a.y = perm(b0, a0, 0x07060302u); a.z = perm(b1, a1, 0x05040100u); a.w = perm(b1, a1, 0x07060302u);
    b.x = perm(b2, a2, 0x05040100u); b.y = perm(b2, a2, 0x07060302u); b.z = perm(b3, a3, 0x05040100u); b.w = perm(b3, a3, 0x07060302u);
}
__device__ __forceinline__ void frag4(const v2u* r, v4u* f) {
    unsigned o[4][4];
#pragma unroll
    for (int p = 0; p < 4; ++p) {
        const unsigned ax = r[2 * p].x, ay = r[2 * p].y, bx = r[2 * p + 1].x, by = r[2 * p + 1].y;
        o[0][p] = perm(bx, ax, 0x05040100u); o[1][p] = perm(bx, ax, 0x07060302u); o[2][p] = perm(by, ay, 0x05040100u); o[3][p] = perm(by, ay, 0x07060302u);
    }
#pragma unroll
    for (int n = 0; n < 4; ++n) { f[n] = v4u{o[n][0], o[n][1], o[n][2], o[n][3]}; PIN(f[n]); }
}
__device__ __forceinline__ void dot8(float (&acc)[8], const v4u& a, const v4u& b, unsigned xp) {
    const unsigned a0 = a.x, a1 = a.y, a2 = a.z, a3 = a.w, b0 = b.x, b1 = b.y, b2 = b.z, b3 = b.w;
    acc[0] = dot2(a0, xp, acc[0]); acc[1] = dot2(a1, xp, acc[1]); acc[2] = dot2(a2, xp, acc[2]); acc[3] = dot2(a3, xp, acc[3]);
    acc[4] = dot2(b0, xp, acc[4]); acc[5] = dot2(b1, xp, acc[5]); acc[6] = dot2(b2, xp, acc[6]); acc[7] = dot2(b3, xp, acc[7]);
}
__device__ __forceinline__ void dot8z(float (&acc)[8], const v4u& a, unsigned slo, unsigned shi) {
    const unsigned a0 = a.x, a1 = a.y, a2 = a.z, a3 = a.w;
    acc[0] = dot2(a0, slo, acc[0]); acc[1] = dot2(a0, shi, acc[1]); acc[2] = dot2(a1, slo, acc[2]); acc[3] = dot2(a1, shi, acc[3]);
    acc[4] = dot2(a2, slo, acc[4]); acc[5] = dot2(a2, shi, acc[5]); acc[6] = dot2(a3, slo, acc[6]); acc[7] = dot2(a3, shi, acc[7]);
}
#define ZERO8(acc) float acc[8] = {0.f, 0.f, 0.f, 0.f, 0.f, 0.f, 0.f, 0.f}; asm volatile("" : "+v"(acc[0]), "+v"(acc[1]), "+v"(acc[2]), "+v"(acc[3]), "+v"(acc[4]), "+v"(acc[5]), "+v"(acc[6]), "+v"(acc[7]))
__device__ __forceinline__ float red8cols(const float (&a)[8], int lane) {
    const float b0 = red32(a[0], a[4]), b1 = red32(a[1], a[5]), b2 = red32(a[2], a[6]), b3 = red32(a[3], a[7]);
    const float c0 = red16(b0, b2), c1 = red16(b1, b3);
    const float d0 = add_x8(c0), d1 = add_x8(c1);
    return (lane & 8) ? d1 : d0;
}
__device__ __forceinline__ u64 rt();
template <bool TAGGED> __device__ __forceinline__ void cue(rsrc_t rws, int off, unsigned tag, int ticks) {
#pragma unroll 1
    for (int sp = 0; sp < SPIN_MAX; ++sp) {
        MEMBAR();
        const unsigned f = __builtin_amdgcn_readfirstlane(ld32_sc1(rws, off));
        if (TAGGED ? f == tag : f != POISON) break;
        __builtin_amdgcn_s_sleep(3);
    }
    const u64 t0 = rt();
    while (rt() - t0 < (u64)ticks) __builtin_amdgcn_s_sleep(1);
}
__device__ __forceinline__ float silu_bf(float v) { return rb(v * __builtin_amdgcn_rcpf(1.f + __expf(-v))); }
__device__ __forceinline__ u64 rt() { return __builtin_amdgcn_s_memrealtime(); }
#define STAMP(i)
#define STAMP_FLUSH()
#define CYC0()
#define CYC1()

__global__ void __attribute__((aligned(4096))) __launch_bounds__(NT, 2) fused_kernel(const u16* a_x, const u16* a_ckv, const u16* a_wq, const u16* a_wuk, const u16* a_wuv, const u16* a_wo, const u16* a_wg, const u16* a_wu,
                                                      const u16* a_wd, const u16* a_c, const u16* a_wmod, u16* a_out, unsigned char* a_ws) {
    __shared__ __attribute__((aligned(16))) unsigned char smem[163840];
    LAS unsigned char* L = (LAS unsigned char*)smem;
    typedef unsigned v16u __attribute__((ext_vector_type(16)));
    typedef unsigned v8u __attribute__((ext_vector_type(8)));
    v16u ka0; v8u ka1; v2u ka2; u64 pc0;
    asm volatile("s_load_dwordx16 %0, %4, 0x0\n\ts_load_dwordx8 %1, %4, 0x40\n\ts_load_dwordx2 %2, %4, 0x60\n\ts_getpc_b64 %3"
                 : "=&s"(ka0), "=&s"(ka1), "=&s"(ka2), "=&s"(pc0) : "s"(__builtin_amdgcn_kernarg_segment_ptr()));
#define KWAIT() do { SCHED(); asm volatile("s_waitcnt lgkmcnt(0)" : "+s"(ka0), "+s"(ka1), "+s"(ka2)); } while (0)
#define KPTR(v, i) ((u64)v[2 * (i)] | ((u64)v[2 * (i) + 1] << 32))
    const u64 t_start = rt();
    const int bid = blockIdx.x, tid = threadIdx.x, lane = tid & 63, w = __builtin_amdgcn_readfirstlane(tid >> 6);
    const int l15 = lane & 15, kq = lane >> 4, half = lane >> 5;
    const int a = bid & 127, xcd = bid & 7, jj = (bid >> 3) & 7;
    const int hh = 4 * xcd + (jj >> 1), cj = jj & 1;
    const int job0 = bid >= 128 ? 4 : (bid < 64 ? 2 : 0) + (w >> 2);
    int job = job0;
    unsigned tag0, sc[6]; v4u s1w[6];
    const int s1t = bid - 64, kp = (s1t * 57) >> 10, nt1 = s1t - 18 * kp;
    constexpr int UKL = 81920, HROW = 147456, QROW = HROW + 1152, SCR = 155648;
    asm volatile("" : "+s"(job));
    if (job == 0) {
        const int r1 = 192 * kp + 48 * w + (lane >> 3);
        int s0 = nt1 * 128 + r1 * 6912 + (lane & 7) * 16, s1 = r1 * 2;
        PIN(s0); PIN(s1);
        if (bid >= 120 && tid < CODE_BYTES / 128) {
            asm volatile("s_mov_b32 m0, %2\n\ts_nop 0\n\tglobal_load_lds_dword %0, %1" :: "v"(tid * 128), "s"(pc0 & ~4095ull), "i"(SCR) : "memory");
        }
        KWAIT();
        const rsrc_t rmod = RSRC(KPTR(ka1, 2), M * 6 * M * 2), rc = RSRC(KPTR(ka1, 1), M * 2);
#pragma unroll
        for (int i = 0; i < 6; ++i) asm volatile("buffer_load_ushort %0, %1, %2, 0 offen offset:%3" : "=v"(sc[i]) : "v"(s1), "s"(rc), "i"(16 * i) : "memory");
        asm volatile("global_load_dword %0, %1, %2 sc1" : "=v"(tag0) : "v"(WS_EPOCH), "s"(KPTR(ka2, 0)) : "memory");
#pragma unroll
        for (int i = 0; i < 6; ++i) asm volatile("buffer_load_dwordx4 %0, %1, %2, %3 offen nt" : "=v"(s1w[i]) : "v"(s0), "s"(rmod), "s"(8 * i * 6912) : "memory");
        SCHED();
        RAWBAR();
        STAMP(10);
    }
    int uko, uks, ukl;
#define UK_LOADS(lo, hi, step) do { const rsrc_t rk = RSRC(KPTR(ka0, 3), DC * 8192); \
        _Pragma("unroll 1") for (int i_ = (lo); i_ < (hi); ++i_) \
            dma16(rk, ukl + i_ * (step), uko ^ ((i_ & 3) << 6), uks + i_ * 32768); } while (0)
    asm volatile("" : "+s"(job));
    if (job == 3) {
        uko = kq * 8192 + ((l15 ^ kq) * 16); uks = (256 * cj + 64 * (w - 4)) * 8192 + hh * 256; ukl = UKL + (w - 4) * 16384;
        PIN(uko);
        KWAIT();
        UK_LOADS(0, 16, 1024);
        RAWBAR();
    }
    int kvo, kvs, kvl;
#define KV_LOADS(n, W) do { const rsrc_t rkv = RSRC(KPTR(ka0, 1), SK * DC * 2); \
        _Pragma("unroll 1") for (int i_ = 0; i_ < (n); ++i_) { \
            dma16(rkv, kvl, kvo, kvs); \
            kvo ^= 32; kvs = KVMAP ? ((kvs + 1048576) & ~0xFE000) | ((kvs + KVROT * 8192) & 0xFE000) : kvs + 8192; kvl += 8192; \
            asm volatile("s_waitcnt vmcnt(%0)" :: "i"((W) - 1) : "memory"); } } while (0)
    asm volatile("" : "+s"(job));
    if (job == 4 || job == 1) {
        int po = xcd * 1024 + lane * 16;
        PIN(po);
        KWAIT();
        if (job == 1) RAWBAR();
        const int n1 = job == 1 ? 896 : 736, ns = job == 1 ? 32 : 128;
#pragma unroll 1
        for (int n = job == 1 ? 704 + 8 * w + jj : 16 * w + (a >> 3); n < n1; n += ns) {
            const bool uk = n < 320;
            dma16(RSRC(uk ? KPTR(ka0, 3) : KPTR(ka0, 2), M * 8192), SCR + w * 1024, po, (uk ? 16 * (n & 28) + 24 + 4 * (n >> 5) + (n & 3) : n - 320) * 8192);
            if (job == 4) asm volatile("s_waitcnt vmcnt(%0)" :: "i"(PFW - 1) : "memory");
        }
        if (job == 1) RAWBAR();
    }
    asm volatile("" : "+s"(job));
    if (job == 2) {
        KWAIT();
        const unsigned e = ld32_sc1(RSRC(KPTR(ka2, 0), WS_BYTES), WS_EPOCH);
        *(LAS v4u*)(L + SCR + tid * 16) = __builtin_amdgcn_raw_buffer_load_b128(RSRC(KPTR(ka0, 0), M * 2), tid * 16, 0, 0);
        *(LAS unsigned*)(L + SCR + 4096) = e + 1u;
        LGKM0();
    }
    asm volatile("" : "+s"(job));
    if (job == 0) {
        const rsrc_t rws = RSRC(KPTR(ka2, 0), WS_BYTES);
        if (s1t >= 54 && s1t < 63) {
            const v4u poison = {POISON, POISON, POISON, POISON};
            const int u = (s1t - 54) * 4 + w;
#pragma unroll 1
            for (int i = 0; i < 8; ++i) __builtin_amdgcn_raw_buffer_store_b128(poison, rws, lane * 16, WS_QA + u * 8192 + i * 1024, AUX_SC1);
            asm volatile("s_waitcnt vmcnt(0)" : "+v"(tag0) :: "memory");
            __builtin_amdgcn_raw_buffer_store_b32(tag0 + 1u, rws, lane * 4, WS_CL + u * 256, AUX_SC1);
        }
        ZERO8(acc);
        int ra = (w * 64 + (lane & 7) * 8 + (lane >> 3)) * 4, rr = l15 * 16, so = (s1t < 54 && lane < 16) ? WS_H1 + kp * 4608 + (nt1 * 16 + lane) * 16 : OOB;
        PIN(ra); PIN(rr); PIN(so);
        SCHED();
        unsigned slo[6], shi[6];
        asm volatile("s_waitcnt vmcnt(6)" : "+v"(sc[0]), "+v"(sc[1]), "+v"(sc[2]), "+v"(sc[3]), "+v"(sc[4]), "+v"(sc[5]), "+v"(tag0));
        tag0 += 1u; PIN(tag0);
#pragma unroll
        for (int i = 0; i < 6; ++i) { shi[i] = f2u(silu_bf(bflo(sc[i]))); slo[i] = shi[i] >> 16; PIN(shi[i]); PIN(slo[i]); }
        SCHED();
#pragma unroll
        for (int i = 0; i < 6; ++i) { asm volatile("s_waitcnt vmcnt(%1)" : "+v"(s1w[i]) : "i"(5 - i)); dot8z(acc, s1w[i], slo[i], shi[i]); SCHED(); }
        *(LAS float*)(L + ra) = red8cols(acc, lane);
        BAR();
        if (w == 0) {
            LAS v4f* R = (LAS v4f*)(L + rr);
            const v4f s = (R[0] + R[16]) + (R[32] + R[48]);
            st_sc1(pack4(s.x, s.y, s.z, s.w, tag0), rws, so);
        }
    }
    const u64 x = KPTR(ka0, 0), ckv = KPTR(ka0, 1), wq = KPTR(ka0, 2), wuv = KPTR(ka0, 4), wo = KPTR(ka0, 5), wg = KPTR(ka0, 6), wu = KPTR(ka0, 7);
    const u64 wd = KPTR(ka1, 0), cc = KPTR(ka1, 1), wmod = KPTR(ka1, 2);
    GAS u16* out = (GAS u16*)KPTR(ka1, 3);
    GAS unsigned char* ws = (GAS unsigned char*)KPTR(ka2, 0);
    const rsrc_t rws = RSRC((u64)ws, WS_BYTES), rx = RSRC(x, M * 2), rmod = RSRC(wmod, M * 6 * M * 2), rc = RSRC(cc, M * 2);
    SCHED();
    STAMP(9);
    asm volatile("" : "+s"(job));

    if (job < 4) {
        const int b = bid;
        LAS float* red = (LAS float*)L;
        const int clo = WS_CL + (lane < 36 ? lane : 0) * 256;
        unsigned tag;
        const bool slowf = SLOW_EARLY && b >= 56 && ((((unsigned)(wg >> 7) + b - 56) & 7) == 3 || (((unsigned)(wu >> 7) + b - 56) & 7) == 3);
        if (job < 2) {
            RAWBAR();
            if (CH_DELAY) { while (rt() - t_start < (u64)CH_DELAY) __builtin_amdgcn_s_sleep(1); }
            const rsrc_t rkv = RSRC(ckv, SK * DC * 2);
#pragma unroll 1
            for (int i = slowf ? 32 : CH0 / 4; i < 32; ++i) {
                dma16(rkv, SCR + w * 1024, lane * 16, (i >> 1) * 1048576 + (((xcd + 8 * (2 * jj + (i & 1)) + KVROT * (i >> 1)) & 127) * 8 + w) * 1024);
                asm volatile("s_waitcnt vmcnt(%0)" :: "i"(CHW - 1) : "memory");
            }
            tag = ld32_sc1(rws, WS_EPOCH) + 1u;
            if (w == 0) {
#pragma unroll 1
                for (int sp = 0; sp < SPIN_MAX; ++sp) { MEMBAR(); if (__all(ld32_sc1(rws, clo) == tag)) break; }
            }
            DRAIN(); RAWBAR();
            STAMP(5);
        } else if (job == 2) {
            v4u bf[2][18];
            {
                const rsrc_t rq = RSRC(wq, M * 8192);
                const int qv = kq * 8192 + hh * 256 + ((l15 ^ (2 * kq) ^ ((w & 2) << 2)) * 16);
                const int q = l15 >> 2, p = lane & 3;
                int t0 = (8 * kq + q) * 256 + (((4 * w + (p >> 1)) ^ (2 * q) ^ ((kq & 1) << 3)) * 16) + 8 * (p & 1), t1 = t0 ^ 32;
                PIN(t0); PIN(t1);
#define WQ_LOADS(lo, hi) _Pragma("unroll 1") for (int i_ = (lo); i_ < (hi); ++i_) \
                    dma16(rq, (4 * i_ + w) * 1024, qv, (4 * i_ + w) * 32768)
#define WQ_FRAGS(lo, hi) _Pragma("unroll") for (int k = (lo); k < (hi); ++k) _Pragma("unroll") for (int m = 0; m < 2; ++m) { \
                    const int tb = (m ? t1 : t0) + (k / 6) * 49152; \
                    const v4s lo_ = __builtin_amdgcn_ds_read_tr16_b64_v4i16((LAS v4s*)(L + tb + (k % 6) * 8192)); \
                    const v4s hi_ = __builtin_amdgcn_ds_read_tr16_b64_v4i16((LAS v4s*)(L + tb + (k % 6) * 8192 + 1024)); \
                    bf[m][k] = __builtin_bit_cast(v4u, __builtin_shufflevector(lo_, hi_, 0, 1, 2, 3, 4, 5, 6, 7)); }
                RAWBAR();
                WQ_LOADS(0, 20);
                RAWBAR();
                WQ_LOADS(20, 36);
                asm volatile("s_waitcnt vmcnt(16)\n\ts_barrier" ::: "memory");
                WQ_FRAGS(0, 10);
                SCHED();
                asm volatile("s_waitcnt vmcnt(0)\n\ts_barrier" ::: "memory");
                STAMP(12);
                WQ_FRAGS(10, 18);
#pragma unroll
                for (int m = 0; m < 2; ++m)
#pragma unroll
                    for (int k = 0; k < 18; ++k) PIN(bf[m][k]);
            }
            tag = *(LAS unsigned*)(L + SCR + 4096); PIN(tag);
            int aa = HROW + kq * 16, qa = QROW + (32 * w + l15) * 2;
            PIN(aa); PIN(qa);
            STAMP(11);
            RAWBAR();
            STAMP(2);
            v4f a0 = {0.f, 0.f, 0.f, 0.f}, a1 = {0.f, 0.f, 0.f, 0.f};
            v4u av[18];
#pragma unroll
            for (int k = 0; k < 6; ++k) av[k] = *(LAS v4u*)(L + aa + k * 64);
            SCHED();
#pragma unroll
            for (int k = 0; k < 18; ++k) {
                a0 = mfma16(av[k], bf[0][k], a0); a1 = mfma16(av[k], bf[1][k], a1);
                if (k + 6 < 18) av[k + 6] = *(LAS v4u*)(L + aa + (k + 6) * 64);
                SCHED();
            }
            const unsigned qp = pkbf(a0.x, a1.x);
            *(LAS u16*)(L + qa) = (u16)qp; *(LAS u16*)(L + qa + 32) = (u16)(qp >> 16);
            BAR();
            STAMP(3);
            RAWBAR();
        } else {
            const int v = w - 4, j36 = lane < 36 ? lane : 35;
            DRAIN();
            STAMP(10);
            v4u uf[4][4];
#pragma unroll
            for (int t = 0; t < 4; ++t)
#pragma unroll
                for (int k = 0; k < 4; ++k) { uf[t][k] = *(LAS v4u*)(L + UKL + v * 16384 + (16 * t + l15) * 256 + (((4 * k + kq) ^ l15) * 16)); }
#pragma unroll
            for (int t = 0; t < 4; ++t)
#pragma unroll
                for (int k = 0; k < 4; ++k) PIN(uf[t][k]);
            BAR();
            float n0, n1, n2, n3;
            const v4u xa = *(LAS v4u*)(L + SCR + lane * 16);
            const unsigned xb = *(LAS unsigned*)(L + SCR + 1024 + lane * 4);
            const v2u xm = *(LAS v2u*)(L + SCR + (36 * v + j36) * 8);
            {
                const unsigned a0 = xa.x, a1 = xa.y, a2 = xa.z, a3 = xa.w, m0 = xm.x, m1 = xm.y;
                float vv[10] = {bflo(a0), bfhi(a0), bflo(a1), bfhi(a1), bflo(a2), bfhi(a2), bflo(a3), bfhi(a3), bflo(xb), bfhi(xb)};
                float s = 0.f;
#pragma unroll
                for (int i = 0; i < 10; ++i) s += vv[i];
                const float mean = wave_sum(s) * (1.f / M);
                float q = 0.f;
#pragma unroll
                for (int i = 0; i < 8; ++i) q += (vv[i] - mean) * (vv[i] - mean);
                if (lane < 32) q += (vv[8] - mean) * (vv[8] - mean) + (vv[9] - mean) * (vv[9] - mean);
                const float var = rb(wave_sum(q) * (1.f / M));
                const float den = rb(__builtin_amdgcn_sqrtf(rb(var + 1e-5f))), mb = rb(mean);
                n0 = rb(rb(bflo(m0) - mb) / den); n1 = rb(rb(bfhi(m0) - mb) / den); n2 = rb(rb(bflo(m1) - mb) / den); n3 = rb(rb(bfhi(m1) - mb) / den);
            }
            tag = *(LAS unsigned*)(L + SCR + 4096);
            int o = WS_H1 + (36 * v + j36) * 16, hw = HROW + (36 * v + j36) * 8, qr = QROW + kq * 16, fo = v == 0 ? clo : WS_CL;
            int qs = l15 < 4 ? WS_QA + hh * 1024 + (256 * cj + 64 * v + 16 * l15 + 4 * kq) * 2 : OOB;
            PIN(n0); PIN(n1); PIN(n2); PIN(n3); PIN(tag); PIN(o); PIN(hw); PIN(qr); PIN(qs); PIN(fo);
            STAMP(11);
            RAWBAR();
            v4u g[6]; unsigned cf;
#define H1_LOADS() do { _Pragma("unroll") for (int i = 0; i < 6; ++i) g[i] = __builtin_amdgcn_raw_buffer_load_b128(rws, o, 2304 * i, AUX_SC1); cf = ld32_sc1(rws, fo); } while (0)
#define H1_OK() __all(g[0].w == tag && g[1].w == tag && g[2].w == tag && g[3].w == tag && g[4].w == tag && g[5].w == tag && cf == tag)
            H1_LOADS();
            SCHED();
            RAWBAR();
            if (__builtin_expect(!H1_OK(), 0)) {
#pragma unroll 1
                for (int sp = 0; sp < SPIN_MAX; ++sp) { MEMBAR(); H1_LOADS(); if (H1_OK()) break; }
            }
            STAMP(12);
            {
                const v4f sa = unpack4n(g[0]) + unpack4n(g[2]) + unpack4n(g[4]), ca = unpack4n(g[1]) + unpack4n(g[3]) + unpack4n(g[5]);
                const unsigned s01 = pkbf(sa.x, sa.y), s23 = pkbf(sa.z, sa.w), c01 = pkbf(ca.x, ca.y), c23 = pkbf(ca.z, ca.w);
                const unsigned d01 = pkbf(1.f + bflo(c01), 1.f + bfhi(c01)), d23 = pkbf(1.f + bflo(c23), 1.f + bfhi(c23));
                const unsigned t01 = pkbf(n0 * bflo(d01), n1 * bfhi(d01)), t23 = pkbf(n2 * bflo(d23), n3 * bfhi(d23));
                *(LAS v2u*)(L + hw) = v2u{pkbf(bflo(t01) + bflo(s01), bfhi(t01) + bfhi(s01)), pkbf(bflo(t23) + bflo(s23), bfhi(t23) + bfhi(s23))};
            }
            BAR();
            RAWBAR();
            STAMP(4);
            {
                v4u qf[4];
#pragma unroll
                for (int k = 0; k < 4; ++k) qf[k] = *(LAS v4u*)(L + qr + k * 64);
                v4f y[4] = {};
#pragma unroll
                for (int t = 0; t < 4; ++t)
#pragma unroll
                    for (int k = 0; k < 4; ++k) y[t] = mfma16(uf[t][k], qf[k], y[t]);
                const v4f z = ((lane & 2) ? ((lane & 1) ? y[3] : y[2]) : ((lane & 1) ? y[1] : y[0])) * (0.08838834764831845f * 1.4426950408889634f);
                st64_sc1(v2u{pkbf(z.x, z.y), pkbf(z.z, z.w)}, rws, qs);
            }
            STAMP(5);
            RAWBAR();
        }
        if (!slowf) { const u64 dl = b >= 56 ? LATE_F : LATE_DELAY; while (rt() - t_start < dl) __builtin_amdgcn_s_sleep(1); }
        if (b >= 56) {
            const int f = b - 56;
            constexpr int FA = 147456, FS = FA + 4 * 1152, FG = FS + 64, FD = 155648;
            if (w < 4) {
                {
                    const rsrc_t rg = RSRC((w & 2) ? wu : wg, M * FF * 2);
                    const int r8 = lane >> 3, base = (288 * (w & 1) + r8) * 9216 + f * 128;
                    const int v0 = base + (((lane & 7) ^ (r8 & 2)) * 16), v1 = base + (((lane & 7) ^ ((r8 & 2) | 4)) * 16);
#pragma unroll
                    for (int i = 0; i < 36; ++i) {
                        dma16(rg, w * 36864 + i * 1024, (i & 1) ? v1 : v0, 8 * i * 9216);
                        if (i < FN0 && i >= FW0 - 1) asm volatile("s_waitcnt vmcnt(%0)" :: "i"(FW0 - 1) : "memory");
                        if (i == 30) RAWBAR();
                    }
                    const rsrc_t rd = RSRC(wd, M * FF * 2);
                    const int db = (64 * f + 16 * w + r8) * 1152 + 1024;
                    dma16(rd, FD + w * 2048, db + (((lane & 7) ^ (r8 & 2)) * 16), 0);
                    dma16(rd, FD + w * 2048 + 1024, db + (((lane & 7) ^ ((r8 & 2) | 4)) * 16), 8 * 1152);
                    DRAIN();
                }
                RAWBAR();
                STAMP(6);
                v4u bf[2][18], ef[2];
                {
                    const int q = l15 >> 2, p = lane & 3;
                    const int ta = (8 * kq + q) * 128 + (((2 * w + (p >> 1)) ^ ((q & 2) | ((kq & 1) << 2))) * 16) + 8 * (p & 1);
#pragma unroll
                    for (int m = 0; m < 2; ++m)
#pragma unroll
                        for (int k = 0; k < 18; ++k) {
                            int tb = ta + m * 73728 + (k >= 9 ? 36864 : 0);
                            const v4s lo = __builtin_amdgcn_ds_read_tr16_b64_v4i16((LAS v4s*)(L + tb + (k % 9) * 4096));
                            const v4s hi = __builtin_amdgcn_ds_read_tr16_b64_v4i16((LAS v4s*)(L + tb + (k % 9) * 4096 + 512));
                            bf[m][k] = __builtin_bit_cast(v4u, __builtin_shufflevector(lo, hi, 0, 1, 2, 3, 4, 5, 6, 7));
                        }
#pragma unroll
                    for (int k = 0; k < 2; ++k) {
                        const v4s lo = __builtin_amdgcn_ds_read_tr16_b64_v4i16((LAS v4s*)(L + (ta + FD) + k * 4096));
                        const v4s hi = __builtin_amdgcn_ds_read_tr16_b64_v4i16((LAS v4s*)(L + (ta + FD) + k * 4096 + 512));
                        ef[k] = __builtin_bit_cast(v4u, __builtin_shufflevector(lo, hi, 0, 1, 2, 3, 4, 5, 6, 7)); PIN(ef[k]);
                    }
#pragma unroll
                    for (int m = 0; m < 2; ++m)
#pragma unroll
                        for (int k = 0; k < 18; ++k) PIN(bf[m][k]);
                }
                int aa = FA + (l15 == 0 ? 0 : (l15 == 4 ? 1152 : (l15 == 8 ? 2304 : 3456))) + kq * 16, ga = FG + (16 * w + l15) * 2;
                int gra = FG + kq * 16, ho = l15 == 0 ? WS_H6 + f * 2304 + 2048 + w * 64 + kq * 16 : OOB;
                PIN(aa); PIN(ga); PIN(gra); PIN(ho);
                STAMP(13);
                RAWBAR();
                STAMP(7);
                v4f ag = {0.f, 0.f, 0.f, 0.f}, au = {0.f, 0.f, 0.f, 0.f};
                v4u av[18];
#pragma unroll
                for (int k = 0; k < 6; ++k) av[k] = *(LAS v4u*)(L + aa + k * 64);
                SCHED();
#pragma unroll
                for (int k = 0; k < 18; ++k) {
                    ag = mfma16(av[k], bf[0][k], ag); au = mfma16(av[k], bf[1][k], au);
                    if (k + 6 < 18) av[k + 6] = *(LAS v4u*)(L + aa + (k + 6) * 64);
                    if (k == 5) RAWBAR();
                    if (k == 13) RAWBAR();
                    SCHED();
                }
                const v2f st = *(LAS v2f*)(L + FS + 32);
                const float cf = kq == 0 ? st.x : (kq == 1 ? -st.y : 1.f);
                float z = red32(ag.x * cf, au.x * cf); z = red16(z, z);
                auto sw = __builtin_amdgcn_permlane32_swap(f2u(z), f2u(z), false, false);
                const unsigned zg = sw[0], zu = sw[1];
                const unsigned gu = pkbf(u2f(zg), u2f(zu));
                const float a1 = bflo(gu), a2 = bfhi(gu);
                constexpr float K0 = -2.f * 1.4426950408889634f * 0.7978845608028654f, K1 = K0 * 0.044715f;
                const float e = __builtin_amdgcn_exp2f(a1 * (K0 + K1 * a1 * a1));
                const float ge = rb(a1 * __builtin_amdgcn_rcpf(1.f + e));
                *(LAS u16*)(L + ga) = (u16)pkbf(ge * a2, 0.f);
                BAR();
                STAMP(14);
                const v4u g0 = *(LAS v4u*)(L + gra), g1 = *(LAS v4u*)(L + gra + 64);
                const v4f y0 = {0.f, 0.f, 0.f, 0.f};
                st_sc1(__builtin_bit_cast(v4u, mfma16(ef[1], g1, mfma16(ef[0], g0, y0))), rws, ho);
                STAMP(8);
            } else {
                const int v = w - 4;
                v4u dr[2][8];
                {
                    const rsrc_t rd = RSRC(wd, M * FF * 2);
                    const int dofs = (64 * f + 8 * kq) * 1152 + v * 256 + l15 * 16;
                    RAWBAR();
#pragma unroll
                    for (int u = 0; u < 2; ++u)
#pragma unroll
                        for (int e = 0; e < 8; ++e) dr[u][e] = __builtin_amdgcn_raw_buffer_load_b128(rd, dofs, (32 * u + e) * 1152, AUXL);
                    SCHED();
                    RAWBAR();
                }
                float xv[3], gav[3], c1v[3]; int to[3], ua[3];
                {
                    unsigned xr[3], gr[3], cr[3]; v4u c4, s4;
                    const int g16 = tid - 256;
                    const int co = g16 < 72 ? WS_MRB + 2304 + g16 * 16 : (g16 < 144 ? WS_MRB + 1152 + (g16 - 72) * 16 : OOB);
#pragma unroll
                    for (int t = 0; t < 3; ++t) {
                        const bool on = t < 2 || v == 3;
                        const int cg = 16 * (2 * v + t) + l15, m = 4 * cg + 2 * half + (kq & 1);
                        to[t] = on ? WS_T5 + kq * 2304 + cg * 16 : OOB; ua[t] = on ? FA + m * 2 : FG + 128 + lane * 2;
                        xr[t] = __builtin_amdgcn_raw_buffer_load_b16(rx, on ? m * 2 : OOB, 0, 0);
                    }
#pragma unroll 1
                    for (int sp = 0;; ++sp) {
                        MEMBAR();
                        unsigned mx = 0;
#pragma unroll
                        for (int t = 0; t < 3; ++t) {
                            const int mo = (t < 2 || v == 3) ? WS_MRB + ((16 * (2 * v + t) + l15) * 4 + 2 * half) * 2 : OOB;
                            gr[t] = ld32_sc1(rws, mo); cr[t] = ld32_sc1(rws, mo + 2304); mx = umax(umax(mx, gr[t]), cr[t]);
                        }
                        c4 = ld_sc1(rws, co);
                        if (__all(umax(umax(mx, c4.x), c4.z) != POISON) || sp > SPIN_MAX) break;
                        __builtin_amdgcn_s_sleep(8);
                    }
                    if (g16 < 216) *(LAS v4u*)(L + FA + 1152 + g16 * 16) = c4;
#pragma unroll
                    for (int t = 0; t < 3; ++t) {
                        xv[t] = bflo(xr[t]); gav[t] = (kq & 1) ? bfhi(gr[t]) : bflo(gr[t]); c1v[t] = (kq & 1) ? bfhi(cr[t]) : bflo(cr[t]);
                        PIN(xv[t]); PIN(gav[t]); PIN(c1v[t]); PIN(to[t]); PIN(ua[t]);
                    }
                }
                STAMP(6);
                int sa = FS + half * 16 + v * 4, gra = FG + kq * 16, ho = lane < 32 ? WS_H6 + f * 2304 + v * 512 + l15 * 32 + kq * 16 : OOB;
                PIN(sa); PIN(gra); PIN(ho);
                v4u df[2][8];
#pragma unroll
                for (int u = 0; u < 2; ++u) {
#pragma unroll
                    for (int p = 0; p < 4; ++p) interleave(dr[u][2 * p], dr[u][2 * p + 1]);
#pragma unroll
                    for (int n = 0; n < 8; ++n) { df[u][n] = v4u{dr[u][n >> 2][n & 3], dr[u][2 + (n >> 2)][n & 3], dr[u][4 + (n >> 2)][n & 3], dr[u][6 + (n >> 2)][n & 3]}; PIN(df[u][n]); }
                }
                STAMP(13);
                cue<false>(rws, WS_CTX + f * 512 + v * 128, 0, CUE7);
                float x2[3];
#pragma unroll 1
                for (int sp = 0;; ++sp) {
                    MEMBAR();
                    v4u t4[3];
#pragma unroll
                    for (int t = 0; t < 3; ++t) t4[t] = ld_sc1(rws, to[t]);
#pragma unroll
                    for (int t = 0; t < 3; ++t) { const unsigned t0 = t4[t].x, t1 = t4[t].y, t2 = t4[t].z, t3 = t4[t].w; x2[t] = red16(red32(u2f(t0), u2f(t2)), red32(u2f(t1), u2f(t3))); }
                    const float chk = x2[0] + x2[1] + x2[2];
                    if (__all(chk == chk) || sp > SPIN_MAX) break;
                }
                STAMP(7);
#pragma unroll
                for (int t = 0; t < 3; ++t) {
                    x2[t] = rb(xv[t] + rb(gav[t] * rb(x2[t])));
                    *(LAS u16*)(L + ua[t]) = (u16)pkbf(x2[t] * c1v[t], 0.f);
                }
                BAR();
                float s = red32(x2[0] + x2[1] + x2[2], x2[0] * x2[0] + x2[1] * x2[1] + x2[2] * x2[2]); s = row_sum(red16(s, s));
                *(LAS float*)(L + sa) = s;
                BAR();
                if (v == 0) {
                    const float mean = sum4(*(LAS v4f*)(L + FS)) * (1.f / M);
                    const float var = rb(fmaxf(sum4(*(LAS v4f*)(L + FS + 16)) * (1.f / M) - mean * mean, 0.f));
                    const float rs = __builtin_amdgcn_rcpf(rb(__builtin_amdgcn_sqrtf(rb(var + 1e-5f))));
                    *(LAS v2f*)(L + FS + 32) = v2f{rs, rb(mean) * rs};
                }
                BAR();
                RAWBAR();
                STAMP(14);
                const v4u g0 = *(LAS v4u*)(L + gra), g1 = *(LAS v4u*)(L + gra + 64);
                v4f y[8] = {};
#pragma unroll
                for (int n = 0; n < 8; ++n) y[n] = mfma16(g1, df[1][n], mfma16(g0, df[0][n], y[n]));
                const bool up = kq & 1;
                st_sc1(v4u{f2u(up ? y[4].x : y[0].x), f2u(up ? y[5].x : y[1].x), f2u(up ? y[6].x : y[2].x), f2u(up ? y[7].x : y[3].x)}, rws, ho);
                STAMP(8);
            }
        } else if (b >= 20) {
            const int o6 = b - 20, kg = o6 / 9, nt = o6 - 9 * kg;
            const int head = 8 * kg + w;
            v4u of[4][4];
            {
                const rsrc_t ro = RSRC(wo, 4096 * M * 2);
                const int oo = (128 * head + 8 * kq) * 1152 + nt * 128 + l15 * 8;
                v2u orr[4][8];
#pragma unroll
                for (int u = 0; u < 4; ++u)
#pragma unroll
                    for (int e = 0; e < 8; ++e) orr[u][e] = ld64_nt(ro, oo, (32 * u + e) * 1152);
                SCHED();
#pragma unroll
                for (int u = 0; u < 4; ++u) frag4(orr[u], of[u]);
            }
            STAMP(6);
            STAMP(13);
            if (w == 0) cue<true>(rws, WS_ST + (3 * o6) * 256 + 12, __builtin_amdgcn_readfirstlane(tag), CUE6);
            RAWBAR();
            const int co = (l15 < 8 ? WS_CTX + ((head >> 1) * 8 + l15) * 512 + (head & 1) * 256 : OOB) + kq * 16;
            v4u a[4];
#pragma unroll 1
            for (int sp = 0;; ++sp) {
                MEMBAR();
#pragma unroll
                for (int u = 0; u < 4; ++u) a[u] = ld_sc1(rws, co + 64 * u);
                unsigned mx = 0;
#pragma unroll
                for (int u = 0; u < 4; ++u) mx = umax(umax(mx, a[u].x), a[u].z);
                if (__all(mx != POISON) || sp > SPIN_MAX) break;
            }
            STAMP(7);
            v4f acc[4] = {};
#pragma unroll
            for (int u = 0; u < 4; ++u)
#pragma unroll
                for (int n = 0; n < 4; ++n) acc[n] = mfma16(a[u], of[u][n], acc[n]);
            v2f c; c.x = red16(sum4(acc[0]), sum4(acc[2])); c.y = red16(sum4(acc[1]), sum4(acc[3]));
            if (lane < 32) *(LAS v2f*)(red + w * 64 + l15 * 4 + kq * 2) = c;
            BAR();
            if (tid < 32) {
                LAS v2f* R = (LAS v2f*)red + lane;
                const v2f s = ((R[0] + R[32]) + (R[64] + R[96])) + ((R[128] + R[160]) + (R[192] + R[224]));
                st64_sc1(v2u{f2u(s.x), f2u(s.y)}, rws, WS_T5 + kg * 2304 + nt * 256 + lane * 8);
            }
            STAMP(8);
        } else if (b < 18) {
            {
                v4u mw[18]; unsigned mc[18];
#pragma unroll
                for (int i = 0; i < 18; ++i) mc[i] = __builtin_amdgcn_raw_buffer_load_b16(rc, (72 * w + 4 * i + kq) * 2, 0, 0);
#pragma unroll
                for (int i = 0; i < 18; ++i) mw[i] = ld_nt(rmod, (72 * w + 4 * i + kq) * 6912 + 2304 + b * 256 + l15 * 16);
                SCHED();
#pragma unroll
                for (int i = 0; i < 18; ++i) { mc[i] = f2u(silu_bf(bflo(mc[i]))); PIN(mc[i]); }
                ZERO8(acc);
#pragma unroll
                for (int i = 0; i < 18; ++i) dot8z(acc, mw[i], mc[i] >> 16, mc[i]);
                const float b0 = red32(acc[0], acc[4]), b1 = red32(acc[1], acc[5]), b2 = red32(acc[2], acc[6]), b3 = red32(acc[3], acc[7]);
                v2f c; c.x = red16(b0, b2); c.y = red16(b1, b3);
                *(LAS v2f*)(red + w * 128 + l15 * 8 + kq * 2) = c;
                BAR();
                if (tid < 32) {
                    v4f s = *(LAS v4f*)(red + tid * 4);
#pragma unroll
                    for (int k = 1; k < 8; ++k) s += *(LAS v4f*)(red + k * 128 + tid * 4);
                    const int col = 128 * b + 4 * tid;
                    const float one = (col >= 1152 && col < 1728) ? 1.f : 0.f;
                    st64_sc1(v2u{pkbf(rb(s.x) + one, rb(s.y) + one), pkbf(rb(s.z) + one, rb(s.w) + one)}, rws, WS_MRB + col * 2);
                }
                STAMP(6);
            }
            if (w == 0) {
                const int cg = 8 * b + (lane & 7);
                const int m = 4 * cg + 2 * half + (kq & 1);
                v4u t; unsigned gb, gaw;
                const unsigned xr = __builtin_amdgcn_raw_buffer_load_b16(rx, m * 2, 0, 0);
                cue<false>(rws, WS_CTX + (72 + b) * 512, 0, CUE7);
#pragma unroll 1
                for (int sp = 0;; ++sp) {
                    MEMBAR();
                    gb = ld32_sc1(rws, WS_MRB + 3 * 1152 + (m & ~1) * 2); gaw = ld32_sc1(rws, WS_MRB + (m & ~1) * 2);
                    t = ld_sc1(rws, WS_T5 + kq * 2304 + cg * 16);
                    if (__all(umax(umax(t.x, t.z), umax(gb, gaw)) != POISON) || sp > SPIN_MAX) break;
                    __builtin_amdgcn_s_sleep(1);
                }
                const unsigned t0 = t.x, t1 = t.y, t2 = t.z, t3 = t.w;
                float x2 = rb(bflo(xr) + rb(((kq & 1) ? bfhi(gaw) : bflo(gaw)) * rb(red16(red32(u2f(t0), u2f(t2)), red32(u2f(t1), u2f(t3))))));
                float gsel = (kq & 1) ? bfhi(gb) : bflo(gb);
                PIN(x2); PIN(gsel);
                STAMP(7);
                { const u64 t0 = rt(); while (rt() - t0 < (u64)CUE8) __builtin_amdgcn_s_sleep(1); }
                v4f s;
#pragma unroll 1
                for (int sp = 0;; ++sp) {
                    MEMBAR();
                    v4u y[9];
#pragma unroll
                    for (int i = 0; i < 9; ++i) y[i] = ld_sc1(rws, WS_H6 + (8 * i + (lane >> 3)) * 2304 + cg * 16);
                    SCHED();
                    s = __builtin_bit_cast(v4f, y[0]);
#pragma unroll
                    for (int i = 1; i < 9; ++i) s += __builtin_bit_cast(v4f, y[i]);
                    if (__all(s.x == s.x) || sp > SPIN_MAX) break;
                }
                const float yv = rb(add_x8(red16(red32(s.x, s.z), red32(s.y, s.w))));
                const float o = x2 + rb(gsel * yv);
                if ((lane & 8) == 0) out[m] = (u16)pkbf(o, o);
                if (b == 0 && lane == 0) st32_sc1(tag, rws, WS_EPOCH);
                STAMP(8);
            }
        }
    } else {
        unsigned f1, tag;
        int f1o = WS_CL + (lane & 3) * 256; PIN(f1o);
        kvo = (lane ^ (((w & 3) << 2) | ((w >> 2) & 3))) * 16; kvs = (KVMAP == 0 ? a * 128 + w : (KVMAP == 1 ? w * 128 + a : a * 8 + w)) * 1024; kvl = w * 1024;
        if (A_DELAY) { while (rt() - t_start < (u64)A_DELAY) {} }
        STAMP(1);
        KV_LOADS(DMAN0, DMAW0);
        KV_LOADS(12 - DMAN0, DMAW);
        asm volatile("global_load_dword %0, %2, %4 sc1\n\tglobal_load_dword %1, %3, %4 sc1" : "=&v"(f1), "=&v"(tag) : "v"(f1o), "v"(WS_EPOCH), "s"(ws) : "memory");
        KV_LOADS(4, DMAW);
        STAMP(2);
        const int hpair = a >> 3, cq = a & 7;
        unsigned ad[8];
        if (w < 4) {
            const int r = lane & 31, g = ((r & 3) << 2) | ((r >> 2) & 3);
#pragma unroll
            for (int j = 0; j < 8; ++j) { ad[j] = w * 32768 + r * 1024 + (((2 * j + half) ^ g) * 16); PIN(ad[j]); }
        } else {
            const int blk = kq & 1, q = l15 >> 2, p = lane & 3;
#pragma unroll
            for (int j = 0; j < 8; ++j) {
                const int t = j >> 1, tt = j & 1, g = (q << 2) | ((2 * tt + half) & 3);
                ad[j] = (8 * tt + 4 * half + q) * 1024 + (w - 4) * 256 + (((4 * t + 2 * blk + (p >> 1)) ^ g) * 16) + 8 * (p & 1); PIN(ad[j]);
            }
        }
        unsigned qw[4];
#pragma unroll
        for (int i = 0; i < 4; ++i) { qw[i] = QOFF + (4 * w + i) * 1024 + ((lane ^ ((i << 2) | (w & 3))) * 16); PIN(qw[i]); }
        int qo = WS_QA + 4 * w * 1024 + lane * 16; PIN(qo);
        asm volatile("s_waitcnt vmcnt(%2)" : "+v"(f1), "+v"(tag) : "i"(QWAIT) : "memory");
        tag += 1u;
        if (__builtin_expect(!__all(f1 == tag), 0)) {
#pragma unroll 1
            for (int sp = 0; sp < SPIN_MAX; ++sp) { MEMBAR(); if (__all(ld32_sc1(rws, f1o) == tag)) break; }
        }
        STAMP(11);
        v4u q[4];
#pragma unroll
        for (int i = 0; i < 4; ++i) q[i] = ld_sc1(rws, qo + i * 1024);
        SCHED();
#define Q_OK() __all(umax(umax(umax(q[0].x, q[0].z), umax(q[1].x, q[1].z)), umax(umax(q[2].x, q[2].z), umax(q[3].x, q[3].z))) != POISON)
#define Q_TO_LDS() do { \
            asm volatile("" : "+v"(q[0]), "+v"(q[1]), "+v"(q[2]), "+v"(q[3])); \
            if (__builtin_expect(!Q_OK(), 0)) { \
                _Pragma("unroll 1") for (int sp = 0; sp < SPIN_MAX; ++sp) { \
                    MEMBAR(); \
                    v4u qn[4]; \
                    _Pragma("unroll") for (int i = 0; i < 4; ++i) qn[i] = ld_sc1(rws, umax(q[i].x, q[i].z) == POISON ? qo + i * 1024 : OOB); \
                    _Pragma("unroll") for (int i = 0; i < 4; ++i) q[i] = umax(q[i].x, q[i].z) == POISON ? qn[i] : q[i]; \
                    if (Q_OK()) break; \
                } \
            } \
            STAMP(12); \
            _Pragma("unroll") for (int i = 0; i < 4; ++i) *(LAS v4u*)(L + qw[i]) = q[i]; \
        } while (0)
        constexpr int OST = QOFF + 16384, OFL = QOFF + 15360;
        RAWBAR();
        if (w < 4) {
            v4u kf[32], qf[32];
            if (w < 3) {
#pragma unroll
                for (int s = 0; s < 32; ++s) kf[s] = *(LAS v4u*)(L + ad[s & 7] + (s >> 3) * 256);
            }
            int mxr = QOFF + 8192 + (lane & 31) * 16, mxw = mxr + w * 4, pwa = QOFF + 2 * w * 1024 + lane * 16;
            PIN(mxr); PIN(mxw); PIN(pwa);
            unsigned aq[8];
#pragma unroll
            for (int j = 0; j < 8; ++j) { aq[j] = ad[j] + (QOFF - w * 32768); PIN(aq[j]); }
            Q_TO_LDS();
            BAR();
            STAMP(3);
            v16f acc = {};
            if (w < 3) {
                constexpr int PD = 8;
#pragma unroll
                for (int s = 0; s < PD; ++s) qf[s] = *(LAS v4u*)(L + aq[s & 7] + (s >> 3) * 256);
                SCHED();
#pragma unroll
                for (int s = 0; s < 32; ++s) {
                    acc = mfma32(kf[s], qf[s], acc);
                    if (s + PD < 32) qf[s + PD] = *(LAS v4u*)(L + aq[(s + PD) & 7] + ((s + PD) >> 3) * 256);
                    SCHED();
                }
            } else {
                constexpr int PD = 6;
#pragma unroll
                for (int s = 0; s < PD; ++s) { kf[s] = *(LAS v4u*)(L + ad[s & 7] + (s >> 3) * 256); qf[s] = *(LAS v4u*)(L + aq[s & 7] + (s >> 3) * 256); }
                SCHED();
#pragma unroll
                for (int s = 0; s < 32; ++s) {
                    acc = mfma32(kf[s], qf[s], acc);
                    if (s + PD < 32) { kf[s + PD] = *(LAS v4u*)(L + ad[(s + PD) & 7] + ((s + PD) >> 3) * 256); qf[s + PD] = *(LAS v4u*)(L + aq[(s + PD) & 7] + ((s + PD) >> 3) * 256); }
                    SCHED();
                }
            }
            RAWBAR();
            STAMP(13);
            float m = -3.0e38f;
#pragma unroll
            for (int i = 0; i < 16; ++i) m = fmaxf(m, acc[i]);
            {
                auto sw = __builtin_amdgcn_permlane32_swap(f2u(m), f2u(m), false, false); const unsigned x0 = sw[0], x1 = sw[1];
                m = __builtin_ceilf(fmaxf(fmaxf(m, u2f(x0)), u2f(x1)));
            }
            *(LAS float*)(L + mxw) = m;
            float p[16];
#pragma unroll
            for (int i = 0; i < 8; ++i) p[i] = __builtin_amdgcn_exp2f(acc[i] - m);
            SCHED();
            BAR();
            const v4f mk = *(LAS v4f*)(L + mxr);
            SCHED();
#pragma unroll
            for (int i = 8; i < 16; ++i) p[i] = __builtin_amdgcn_exp2f(acc[i] - m);
            unsigned pw[8];
#pragma unroll
            for (int i = 0; i < 8; ++i) pw[i] = pkbf(p[2 * i], p[2 * i + 1]);
            SCHED();
            const float mm = fmaxf(fmaxf(fmaxf(fmaxf(m, mk.x), mk.y), mk.z), mk.w);
            const float dm = fminf(mm - m, 255.f);
            const unsigned sub = (unsigned)(dm * 8388736.f);
#pragma unroll
            for (int i = 0; i < 8; ++i) pw[i] = __builtin_bit_cast(unsigned, __builtin_elementwise_sub_sat(__builtin_bit_cast(v2us, pw[i]), __builtin_bit_cast(v2us, sub)));
            *(LAS v4u*)(L + pwa) = v4u{pw[0], pw[1], pw[2], pw[3]};
            *(LAS v4u*)(L + pwa + 1024) = v4u{pw[4], pw[5], pw[6], pw[7]};
            SCHED();
            BAR();
            SCHED();
            STAMP(4);
            float l = 0.f;
#pragma unroll
            for (int i = 0; i < 16; ++i) l += p[i];
            l = red32(l, l) * __builtin_amdgcn_exp2f(-dm);
            *(LAS float*)(L + mxw + 512) = l;
            BAR();
            if (w == 1) {
                const float ls = sum4(*(LAS v4f*)(L + mxr + 512));
                st_sc1(pack4(mm, dpp<0xB1>(mm), ls, dpp<0xB1>(ls), tag), rws, (lane & 33) ? OOB : WS_ST + a * 256 + lane * 8);
            }
            int io = WS_INB + a * 32768 + (32 * w + 8 * kq) * 256 + l15 * 16, sto = WS_ST + (32 * w + (lane & 31)) * 256 + hpair * 16;
            int wtw = QOFF + 12288 + w * 128 + lane * 2, wtr = QOFF + 12288 + w * 128 + (lane & 1) * 64 + kq * 16, mw = QOFF + 12800 + half * 16;
            int cko = lane < 32 ? WS_CK + ((32 * w + lane) * 8 + cq) * 128 + hpair * 8 : OOB;
            int law = QOFF + 13312 + (kq & 1) * 512 + w * 128 + l15 * 8;
            {
                int r0 = OST + w * 2048 + lane * 16, r1 = kq * 1024 + w * 256 + l15 * 16, so = WS_INB + ((lane >> 3) * 8 + 2 * w) * 32768 + a * 256 + (lane & 7) * 16;
                int co = (lane & 6) ? OOB : WS_CK + (a * 8 + 2 * w) * 128 + ((lane >> 3) + 8 * (lane & 1)) * 8, fl = OFL + w * 4;
                PIN(r0); PIN(r1); PIN(so); PIN(co); PIN(fl);
                unsigned c0 = 0, c1 = 0;
#pragma unroll
                for (int t = 0; t < 4; ++t) {
                    if (t < 3) {
#pragma unroll 1
                        for (int sp = 0; sp < (1 << 22); ++sp) { MEMBAR(); if (__all(*(volatile LAS unsigned*)(L + fl + t * 16) == tag)) break; }
                        MEMBAR();
                    } else RAWBAR();
                    const v4u o0 = *(LAS v4u*)(L + (t ? r1 + t * 8192 : r0)), o1 = *(LAS v4u*)(L + (t ? r1 + t * 8192 + 4096 : r0 + 1024));
                    __builtin_amdgcn_raw_buffer_store_b128(o0, rws, so + (t & 1) * 128, (t >> 1) * 32768, AUX_SC1);
                    __builtin_amdgcn_raw_buffer_store_b128(o1, rws, so + (t & 1) * 128, (64 + (t >> 1)) * 32768, AUX_SC1);
                    SCHED();
                    c0 += (o0.x + o0.y) + (o0.z + o0.w); c1 += (o1.x + o1.y) + (o1.z + o1.w);
                    if (t & 1) {
                        c0 = addu_dpp<0xB1>(c0); c1 = addu_dpp<0xB1>(c1);
                        unsigned c = addu_dpp<0x4E>((lane & 1) ? c1 : c0);
                        c += (unsigned)__builtin_amdgcn_update_dpp(0, __builtin_amdgcn_update_dpp(0, (int)c, 0x1B, 0xf, 0xf, true), 0x141, 0xf, 0xf, true);
                        st64_sc1(v2u{c, tag}, rws, co + (t >> 1) * 128);
                        c0 = 0; c1 = 0;
                    } else { PIN(c0); PIN(c1); }
                    if (t == 0) { PIN(io); PIN(sto); PIN(wtw); PIN(wtr); PIN(mw); PIN(cko); PIN(law); }
                    SCHED();
                }
                STAMP(5);
            }
            v4u s0 = ld_sc1(rws, sto), d[8];
            PIN(s0);
            if (__builtin_expect(!__all(s0.w == tag), 0)) {
#pragma unroll 1
                for (int sp = 0; sp < SPIN_MAX; ++sp) { MEMBAR(); s0 = ld_sc1(rws, sto); if (__all(s0.w == tag)) break; }
            }
            STAMP(6);
            if (CUE5) { const u64 t0 = rt(); while (rt() - t0 < (u64)CUE5) {} }
#pragma unroll
            for (int e = 0; e < 8; ++e) { d[e] = ld_sc1(rws, io + e * 256); SCHED(); }
            v2u ck = __builtin_amdgcn_raw_buffer_load_b64(rws, cko, 0, AUX_SC1);
            SCHED();
            v4u wa;
            {
                const v4f sv = unpack4(s0);
                const float ms = half ? sv.y : sv.x, lv = half ? sv.w : sv.z;
                *(LAS float*)(L + mw + w * 4) = row_max(max16(ms));
                BAR();
                const v4f mk2 = *(LAS v4f*)(L + mw);
                const float e = rb(__builtin_amdgcn_exp2f(ms - fmaxf(fmaxf(fmaxf(fmaxf(ms, mk2.x), mk2.y), mk2.z), mk2.w)));
                *(LAS u16*)(L + wtw) = (u16)(f2u(e) >> 16);
                float ll = e * lv; ll = row_sum(red16(ll, ll));
                *(LAS float*)(L + mw + 32 + w * 4) = ll;
                LGKM0();
                wa = *(LAS v4u*)(L + wtr);
                PIN(wa);
            }
            STAMP(14);
            v2u lo2;
#define COMBINE(ok) do { \
                v4u lo[4], hi[4]; unsigned sm = 0; \
                _Pragma("unroll") for (int p2 = 0; p2 < 4; ++p2) { \
                    _Pragma("unroll") for (int t = 0; t < 4; ++t) { const unsigned u0 = d[2 * p2][t], u1 = d[2 * p2 + 1][t]; lo[t][p2] = perm(u1, u0, 0x05040100u); hi[t][p2] = perm(u1, u0, 0x07060302u); sm += u0 + u1; } \
                    SCHED(); } \
                const v4f z = {0.f, 0.f, 0.f, 0.f}; float la0[4], la1[4]; \
                _Pragma("unroll") for (int t = 0; t < 4; ++t) { la0[t] = mfma16(wa, lo[t], z).x; la1[t] = mfma16(wa, hi[t], z).y; } \
                sm -= ck.x; \
                sm = addu_dpp<0x124>(addu_dpp<0x128>(addu_dpp<0x4E>(addu_dpp<0xB1>(sm)))); \
                sm = redu16(sm, sm); sm = redu32(sm, sm); \
                ok = __all(sm == 0u && (ck.y == tag || lane >= 32)); \
                const unsigned a0 = pkbf(la0[0], la0[1]), a1 = pkbf(la0[2], la0[3]), b0 = pkbf(la1[0], la1[1]), b1 = pkbf(la1[2], la1[3]); \
                lo2 = (kq & 1) ? v2u{b0, b1} : v2u{a0, a1}; \
            } while (0)
            bool ok;
            COMBINE(ok);
            if (__builtin_expect(!ok, 0)) {
#pragma unroll 1
                for (int sp = 0; sp < SPIN_MAX && !ok; ++sp) {
#pragma unroll 1
                    for (int s2 = 0; s2 < SPIN_MAX && !__all(ck.y == tag || lane >= 32); ++s2) { MEMBAR(); ck = __builtin_amdgcn_raw_buffer_load_b64(rws, cko, 0, AUX_SC1); }
                    MEMBAR();
#pragma unroll
                    for (int e = 0; e < 8; ++e) d[e] = ld_sc1(rws, io + e * 256);
                    COMBINE(ok);
                }
            }
            *(LAS v2u*)(L + law) = lo2;
            STAMP(7);
            BAR();
        } else {
            const int v = w - 4;
            const int col = lane & 31;
            int ob0 = OST + v * 2048 + 2 * half * 128 + col * 4, ob1 = half * 1024 + v * 256 + col * 4, fl = OFL + v * 4, pra = QOFF + lane * 16;
            const rsrc_t rv = RSRC(wuv, DC * 8192);
            int vo = (64 * cq + 8 * kq) * 8192 + hpair * 512 + v * 128 + l15 * 8;
            unsigned ah[8];
#pragma unroll
            for (int j = 0; j < 8; ++j) { ah[j] = ad[j] + 65536; PIN(ah[j]); }
            PIN(ob0); PIN(ob1); PIN(fl); PIN(pra); PIN(vo);
            Q_TO_LDS();
            BAR();
            v2u vr[2][8];
#pragma unroll
            for (int i = 0; i < UVA; ++i) vr[i >> 3][i & 7] = ld64_nt(rv, vo, (32 * (i >> 3) + (i & 7)) * 8192);
            SCHED();
            v4s vb[4][16];
#define VRD(t, k) vb[t][k] = __builtin_amdgcn_ds_read_tr16_b64_v4i16((LAS v4s*)(L + (((k) >> 3) ? ah[2 * (t) + ((k) & 1)] : ad[2 * (t) + ((k) & 1)]) + (((k) >> 1) & 3) * 16384))
#define VREAD(t) _Pragma("unroll") for (int k = 0; k < 16; ++k) VRD(t, k)
            RAWBAR();
            VREAD(0);
            SCHED();
            RAWBAR();
            VREAD(1);
            __builtin_amdgcn_s_setprio(2);
            BAR();
            STAMP(4); CYC0();
            v4u pa[8];
#pragma unroll
            for (int ks = 0; ks < 8; ++ks) pa[ks] = *(LAS v4u*)(L + pra + ks * 1024);
            SCHED();
            v16f acc[4];
#define OSTORE(t, e) (*(LAS unsigned*)(L + ((t) ? ob1 + (t) * 8192 + ((e) >> 1) * 2048 + ((e) & 1) * 128 : ob0 + (4 * ((e) >> 1) + ((e) & 1)) * 128)) = pkbf(acc[t][2 * (e)], acc[t][2 * (e) + 1]))
#pragma unroll
            for (int t = 0; t < 4; ++t) {
#pragma unroll
                for (int ks = 0; ks < 8; ++ks) {
                    const v4u bv = __builtin_bit_cast(v4u, __builtin_shufflevector(vb[t][2 * ks], vb[t][2 * ks + 1], 0, 1, 2, 3, 4, 5, 6, 7));
                    const v16f z = {};
                    acc[t] = mfma32(pa[ks], bv, ks ? acc[t] : z);
                    if (t < 2) { VRD(t + 2, 2 * ks); VRD(t + 2, 2 * ks + 1); }
                    if (t > 0 && ks >= 2 && ks < 6) { OSTORE(t - 1, 2 * (ks - 2)); OSTORE(t - 1, 2 * (ks - 2) + 1); }
                    if (t > 0 && ks == 5) { MEMBAR(); *(volatile LAS unsigned*)(L + fl + (t - 1) * 16) = tag; }
                    if (t == 0 && ks == 5) RAWBAR();
                    SCHED();
                }
            }
            STAMP(13); CYC1();
#pragma unroll
            for (int e = 0; e < 8; ++e) OSTORE(3, e);
            BAR();
            __builtin_amdgcn_s_setprio(0);
            if (UVA < 16) __builtin_amdgcn_s_sleep(3);
#pragma unroll
            for (int i = UVA; i < 16; ++i) vr[i >> 3][i & 7] = ld64_nt(rv, vo, (32 * (i >> 3) + (i & 7)) * 8192);
            SCHED();
            RAWBAR();
            v4u vf[2][4];
            frag4(vr[0], vf[0]); frag4(vr[1], vf[1]);
            int lr = QOFF + 13312 + (v >> 1) * 512 + (lane & 3) * 128 + kq * 16, cxo = lane < 16 ? WS_CTX + a * 512 + v * 128 + l15 * 8 : OOB, lo = QOFF + 12832 + (v >> 1) * 16;
            PIN(lr); PIN(cxo); PIN(lo);
            RAWBAR();
            {
                const v4u a0 = *(LAS v4u*)(L + lr), a1 = *(LAS v4u*)(L + lr + 64);
                const float rl = __builtin_amdgcn_rcpf(sum4(*(LAS v4f*)(L + lo)));
                v4f y[4] = {};
#pragma unroll
                for (int n = 0; n < 4; ++n) y[n] = mfma16(a1, vf[1][n], mfma16(a0, vf[0][n], y[n]));
                st64_sc1(v2u{pkbf(sum4(y[0]) * rl, sum4(y[1]) * rl), pkbf(sum4(y[2]) * rl, sum4(y[3]) * rl)}, rws, cxo);
            }
            STAMP(8);
        }
    }
    STAMP_FLUSH();
}

extern "C" void kernel_launch(void* const* d_in, const int* in_sizes, int n_in, void* d_out, int out_size, void* d_ws, size_t ws_size, hipStream_t stream) {
    fused_kernel<<<256, NT, 0, stream>>>((const u16*)d_in[0], (const u16*)d_in[1], (const u16*)d_in[2], (const u16*)d_in[3], (const u16*)d_in[4], (const u16*)d_in[5],
                                         (const u16*)d_in[7], (const u16*)d_in[8], (const u16*)d_in[9], (const u16*)d_in[10], (const u16*)d_in[11], (u16*)d_out, (unsigned char*)d_ws + WS_BASE);
}
```

```cpp
#include <hip/hip_runtime.h>
#include <hip/hip_bf16.h>

#define STAMPS 1
#define PROBE 1
#define LATE_DELAY 500
#define A_DELAY 0
#define B_DELAY 0
#define CL_DELAY 0
#define K3E 8
#define DMAW 4
#define AUXF 2
#define AUXL 2
#define SLOW_EARLY 1
#define CLB 1
#define S1DUP 0
#define KPF 1
#define KPF_DELAY 0
#define AUXK 2
#define K3LATE 1
#define QHINT 0
#define AUXS 2
#define WQE 5
#define K3A 0
#define DMAW0 DMAW
#define DMAN0 4
#define UVA 10
#define S5DRAIN 1
#define DPF_SLEEP 8
#define UKG 12
#define CH0 80
#define CHW 4
#define CH_DELAY 200
#define PFW 3
#define LATE_F 450
#define FW0 36
#define FN0 0
#define KVMAP 2
#define KVROT 23
#define HINTI 13
#define MOD_EARLY 0
#define CUE5 0
#define CUE6 80
#define CUE7 40
#define CUE8 50
#define QWAIT 2

#define WS_BASE 0
typedef unsigned short u16;
typedef unsigned long long u64;
typedef unsigned v4u __attribute__((ext_vector_type(4)));
typedef unsigned v2u __attribute__((ext_vector_type(2)));
typedef float v4f __attribute__((ext_vector_type(4)));
typedef float v2f __attribute__((ext_vector_type(2)));
typedef float v16f __attribute__((ext_vector_type(16)));
typedef short v4s __attribute__((ext_vector_type(4)));
typedef unsigned short v2us __attribute__((ext_vector_type(2)));
typedef __bf16 v2bf __attribute__((ext_vector_type(2)));
typedef __bf16 v8bf __attribute__((ext_vector_type(8)));
#define LAS __attribute__((address_space(3)))
#define GAS __attribute__((address_space(1)))
typedef __amdgpu_buffer_rsrc_t rsrc_t;

constexpr int M = 576, DC = 512, SK = 16384, FF = 4608;
constexpr int NT = 512;
constexpr int QOFF = 131072;
constexpr int CODE_BYTES = 28416;
constexpr int SPIN_MAX = 1 << 16;
constexpr unsigned POISON = 0xFFFFFFFFu;
constexpr int OOB = 0x40000000;
constexpr int WS_EPOCH = 0;
constexpr int WS_CL = 4096;
constexpr int WS_H1 = WS_CL + 128 * 128;
constexpr int WS_ST = WS_H1 + 3 * 288 * 16;
constexpr int WS_QA = 256 * 1024;
constexpr int WS_PZ = WS_QA + 32768;
constexpr int WS_CTX = WS_PZ;
constexpr int WS_U5 = WS_CTX + 65536;
constexpr int WS_T5 = WS_U5 + 4608;
constexpr int WS_MRB = WS_T5 + 9216;
constexpr int WS_H6 = WS_MRB + 4608;
constexpr int WS_CK = 768 * 1024;
constexpr int WS_INB = 1 << 20;
constexpr int WS_STAMP = WS_INB + (4 << 20);
constexpr int WS_BYTES = WS_STAMP + 256 * 8 * 16 * 4;
static_assert(WS_ST + 32768 <= WS_QA && WS_H6 + 72 * 2304 <= WS_PZ + 8 * 32768 && WS_PZ + 8 * 32768 <= WS_CK && WS_CK + 131072 <= WS_INB, "ws map");
static_assert(WS_H1 % 128 == 0 && WS_ST % 128 == 0 && WS_U5 % 128 == 0 && WS_T5 % 128 == 0 && WS_MRB % 128 == 0 && WS_H6 % 128 == 0, "line aligned");

#define RSRC(p, bytes) __builtin_amdgcn_make_buffer_rsrc((void*)(p), 0, (bytes), 0x00020000)
#define AUX_NT 2
#define AUX_SC1 16
__device__ __forceinline__ v4u ld_nt(rsrc_t r, int off) { return __builtin_amdgcn_raw_buffer_load_b128(r, off, 0, AUXL); }
__device__ __forceinline__ v2u ld64_nt(rsrc_t r, int off, int so) { return __builtin_amdgcn_raw_buffer_load_b64(r, off, so, AUXL); }
__device__ __forceinline__ v4u ld_sc1(rsrc_t r, int off) { return __builtin_amdgcn_raw_buffer_load_b128(r, off, 0, AUX_SC1); }
__device__ __forceinline__ unsigned ld32_sc1(rsrc_t r, int off) { return __builtin_amdgcn_raw_buffer_load_b32(r, off, 0, AUX_SC1); }
__device__ __forceinline__ void st_sc1(v4u v, rsrc_t r, int off) { __builtin_amdgcn_raw_buffer_store_b128(v, r, off, 0, AUX_SC1); }
__device__ __forceinline__ void st64_sc1(v2u v, rsrc_t r, int off) { __builtin_amdgcn_raw_buffer_store_b64(v, r, off, 0, AUX_SC1); }
__device__ __forceinline__ void st32_sc1(unsigned v, rsrc_t r, int off) { __builtin_amdgcn_raw_buffer_store_b32(v, r, off, 0, AUX_SC1); }
__device__ __forceinline__ void dma16(rsrc_t r, int lds, int voff, int soff) {
    asm volatile("s_mov_b32 m0, %0\n\ts_nop 0\n\tbuffer_load_dwordx4 %1, %2, %3 offen nt lds" :: "s"(lds), "v"(voff), "s"(r), "s"(soff) : "memory");
}
#define MEMBAR() asm volatile("" ::: "memory")
#define DRAIN() asm volatile("s_waitcnt vmcnt(0)" ::: "memory")
#define SCHED() __builtin_amdgcn_sched_barrier(0)
#define PIN(x) asm volatile("" : "+v"(x))
#define LGKM0() do { MEMBAR(); __builtin_amdgcn_s_waitcnt(0xC07F); MEMBAR(); } while (0)
#define BAR() do { LGKM0(); asm volatile("s_barrier" ::: "memory"); } while (0)
#define RAWBAR() do { asm volatile("s_barrier" ::: "memory"); } while (0)

__device__ __forceinline__ float u2f(unsigned u) { return __builtin_bit_cast(float, u); }
__device__ __forceinline__ unsigned f2u(float f) { return __builtin_bit_cast(unsigned, f); }
__device__ __forceinline__ float bflo(unsigned u) { return u2f(u << 16); }
__device__ __forceinline__ float bfhi(unsigned u) { return u2f(u & 0xFFFF0000u); }
__device__ __forceinline__ unsigned pkbf(float lo, float hi) { v2f p = {lo, hi}; v2bf q = __builtin_convertvector(p, v2bf); return __builtin_bit_cast(unsigned, q); }
__device__ __forceinline__ float rb(float f) { return bflo(pkbf(f, f)); }
__device__ __forceinline__ float dot2(unsigned a, unsigned b, float c) { return __builtin_amdgcn_fdot2_f32_bf16(__builtin_bit_cast(v2bf, a), __builtin_bit_cast(v2bf, b), c, false); }
__device__ __forceinline__ unsigned perm(unsigned hi, unsigned lo, unsigned sel) { return __builtin_amdgcn_perm(hi, lo, sel); }
__device__ __forceinline__ unsigned umax(unsigned a, unsigned b) { return a > b ? a : b; }
__device__ __forceinline__ v4f mfma16(v4u a, v4u b, v4f c) { return __builtin_amdgcn_mfma_f32_16x16x32_bf16(__builtin_bit_cast(v8bf, a), __builtin_bit_cast(v8bf, b), c, 0, 0, 0); }
__device__ __forceinline__ v16f mfma32(v4u a, v4u b, v16f c) { return __builtin_amdgcn_mfma_f32_32x32x16_bf16(__builtin_bit_cast(v8bf, a), __builtin_bit_cast(v8bf, b), c, 0, 0, 0); }
__device__ __forceinline__ float sum4(v4f a) { return (a.x + a.z) + (a.y + a.w); }
__device__ __forceinline__ v4u pack4(float a, float b, float c, float d, unsigned tag) {
    const unsigned ud = f2u(d);
    v4u g; g.x = perm(f2u(a), ud, 0x07060501u); g.y = perm(f2u(b), ud, 0x07060502u); g.z = perm(f2u(c), ud, 0x07060503u); g.w = tag; return g;
}
__device__ __forceinline__ v4f unpack4(v4u g) {
    const unsigned gx = g.x, gy = g.y, gz = g.z;
    const unsigned t = perm(gy, gx, 0x0c04000cu);
    const unsigned d = perm(gz, t, 0x04020100u);
    v4f r = {u2f(gx & 0xFFFFFF00u), u2f(gy & 0xFFFFFF00u), u2f(gz & 0xFFFFFF00u), u2f(d)}; return r;
}
__device__ __forceinline__ v4f unpack4n(v4u g) {
    const unsigned gx = g.x, gy = g.y, gz = g.z;
    v4f r = {u2f(gx), u2f(gy), u2f(gz), u2f(perm(gz, perm(gy, gx, 0x0c04000cu), 0x04020100u))}; return r;
}
__device__ __forceinline__ float red32(float a, float b) { auto r = __builtin_amdgcn_permlane32_swap(f2u(a), f2u(b), false, false); const unsigned x = r[0], y = r[1]; return u2f(x) + u2f(y); }
__device__ __forceinline__ float red16(float a, float b) { auto r = __builtin_amdgcn_permlane16_swap(f2u(a), f2u(b), false, false); const unsigned x = r[0], y = r[1]; return u2f(x) + u2f(y); }
__device__ __forceinline__ float max32(float a, float b) { auto r = __builtin_amdgcn_permlane32_swap(f2u(a), f2u(b), false, false); const unsigned x = r[0], y = r[1]; return fmaxf(u2f(x), u2f(y)); }
__device__ __forceinline__ float max16(float a) { auto r = __builtin_amdgcn_permlane16_swap(f2u(a), f2u(a), false, false); const unsigned x = r[0], y = r[1]; return fmaxf(u2f(x), u2f(y)); }
template <int CTRL> __device__ __forceinline__ float dpp(float v) { return u2f((unsigned)__builtin_amdgcn_update_dpp(0, (int)f2u(v), CTRL, 0xf, 0xf, true)); }
__device__ __forceinline__ float add_x8(float v) { return v + dpp<0x128>(v); }
__device__ __forceinline__ float add_r4(float v) { return v + dpp<0x124>(v); }
__device__ __forceinline__ float add_x2(float v) { return v + dpp<0x4E>(v); }
__device__ __forceinline__ float add_x1(float v) { return v + dpp<0xB1>(v); }
__device__ __forceinline__ float row_sum(float v) { return add_r4(add_x8(add_x2(add_x1(v)))); }
__device__ __forceinline__ float row_max(float v) { v = fmaxf(v, dpp<0xB1>(v)); v = fmaxf(v, dpp<0x4E>(v)); v = fmaxf(v, dpp<0x128>(v)); return fmaxf(v, dpp<0x124>(v)); }
template <int CTRL> __device__ __forceinline__ unsigned addu_dpp(unsigned v) { return v + (unsigned)__builtin_amdgcn_update_dpp(0, (int)v, CTRL, 0xf, 0xf, true); }
__device__ __forceinline__ unsigned redu16(unsigned a, unsigned b) { auto r = __builtin_amdgcn_permlane16_swap(a, b, false, false); const unsigned x = r[0], y = r[1]; return x + y; }
__device__ __forceinline__ unsigned redu32(unsigned a, unsigned b) { auto r = __builtin_amdgcn_permlane32_swap(a, b, false, false); const unsigned x = r[0], y = r[1]; return x + y; }
__device__ __forceinline__ float wave_sum(float v) { v = row_sum(v); v = red16(v, v); return red32(v, v); }
__device__ __forceinline__ void interleave(v4u& a, v4u& b) {
    const unsigned a0 = a.x, a1 = a.y, a2 = a.z, a3 = a.w, b0 = b.x, b1 = b.y, b2 = b.z, b3 = b.w;
    a.x = perm(b0, a0, 0x05040100u); a.y = perm(b0, a0, 0x07060302u); a.z = perm(b1, a1, 0x05040100u); a.w = perm(b1, a1, 0x07060302u);
    b.x = perm(b2, a2, 0x05040100u); b.y = perm(b2, a2, 0x07060302u); b.z = perm(b3, a3, 0x05040100u); b.w = perm(b3, a3, 0x07060302u);
}
__device__ __forceinline__ void frag4(const v2u* r, v4u* f) {
    unsigned o[4][4];
#pragma unroll
    for (int p = 0; p < 4; ++p) {
        const unsigned ax = r[2 * p].x, ay = r[2 * p].y, bx = r[2 * p + 1].x, by = r[2 * p + 1].y;
        o[0][p] = perm(bx, ax, 0x05040100u); o[1][p] = perm(bx, ax, 0x07060302u); o[2][p] = perm(by, ay, 0x05040100u); o[3][p] = perm(by, ay, 0x07060302u);
    }
#pragma unroll
    for (int n = 0; n < 4; ++n) { f[n] = v4u{o[n][0], o[n][1], o[n][2], o[n][3]}; PIN(f[n]); }
}
__device__ __forceinline__ void dot8(float (&acc)[8], const v4u& a, const v4u& b, unsigned xp) {
    const unsigned a0 = a.x, a1 = a.y, a2 = a.z, a3 = a.w, b0 = b.x, b1 = b.y, b2 = b.z, b3 = b.w;
    acc[0] = dot2(a0, xp, acc[0]); acc[1] = dot2(a1, xp, acc[1]); acc[2] = dot2(a2, xp, acc[2]); acc[3] = dot2(a3, xp, acc[3]);
    acc[4] = dot2(b0, xp, acc[4]); acc[5] = dot2(b1, xp, acc[5]); acc[6] = dot2(b2, xp, acc[6]); acc[7] = dot2(b3, xp, acc[7]);
}
__device__ __forceinline__ void dot8z(float (&acc)[8], const v4u& a, unsigned slo, unsigned shi) {
    const unsigned a0 = a.x, a1 = a.y, a2 = a.z, a3 = a.w;
    acc[0] = dot2(a0, slo, acc[0]); acc[1] = dot2(a0, shi, acc[1]); acc[2] = dot2(a1, slo, acc[2]); acc[3] = dot2(a1, shi, acc[3]);
    acc[4] = dot2(a2, slo, acc[4]); acc[5] = dot2(a2, shi, acc[5]); acc[6] = dot2(a3, slo, acc[6]); acc[7] = dot2(a3, shi, acc[7]);
}
#define ZERO8(acc) float acc[8] = {0.f, 0.f, 0.f, 0.f, 0.f, 0.f, 0.f, 0.f}; asm volatile("" : "+v"(acc[0]), "+v"(acc[1]), "+v"(acc[2]), "+v"(acc[3]), "+v"(acc[4]), "+v"(acc[5]), "+v"(acc[6]), "+v"(acc[7]))
__device__ __forceinline__ float red8cols(const float (&a)[8], int lane) {
    const float b0 = red32(a[0], a[4]), b1 = red32(a[1], a[5]), b2 = red32(a[2], a[6]), b3 = red32(a[3], a[7]);
    const float c0 = red16(b0, b2), c1 = red16(b1, b3);
    const float d0 = add_x8(c0), d1 = add_x8(c1);
    return (lane & 8) ? d1 : d0;
}
__device__ __forceinline__ u64 rt();
template <bool TAGGED> __device__ __forceinline__ void cue(rsrc_t rws, int off, unsigned tag, int ticks) {
#pragma unroll 1
    for (int sp = 0; sp < SPIN_MAX; ++sp) {
        MEMBAR();
        const unsigned f = __builtin_amdgcn_readfirstlane(ld32_sc1(rws, off));
        if (TAGGED ? f == tag : f != POISON) break;
        __builtin_amdgcn_s_sleep(3);
    }
    const u64 t0 = rt();
    while (rt() - t0 < (u64)ticks) __builtin_amdgcn_s_sleep(1);
}
__device__ __forceinline__ float silu_bf(float v) { return rb(v * __builtin_amdgcn_rcpf(1.f + __expf(-v))); }
__device__ __forceinline__ u64 rt() { return __builtin_amdgcn_s_memrealtime(); }
#define STAMP(i) do { SCHED(); ts[i] = (unsigned)rt(); SCHED(); } while (0)
__device__ __forceinline__ int stamp_group_d(int b) { return b >= 128 ? 0 : (b >= 64 ? 1 : (b >= 56 ? 4 : (b >= 18 ? 2 : 3))); }
#define STAMP_FLUSH() do { if (stamp_group_d(blockIdx.x) == 2 && w == 4) { const unsigned lim = 4000u + (ts[10] ? 4u * (ts[10] - ts[0]) : 0u); while ((unsigned)rt() - ts[0] < lim) __builtin_amdgcn_s_sleep(2); } } while (0)
#define CYC0() SCHED(); const unsigned cyc0_ = (unsigned)__builtin_amdgcn_s_memtime(); SCHED()
#define CYC1() do { SCHED(); ts[15] = (unsigned)__builtin_amdgcn_s_memtime() - cyc0_; SCHED(); } while (0)

__global__ void __attribute__((aligned(4096))) __launch_bounds__(NT, 2) fused_kernel(const u16* a_x, const u16* a_ckv, const u16* a_wq, const u16* a_wuk, const u16* a_wuv, const u16* a_wo, const u16* a_wg, const u16* a_wu,
                                                      const u16* a_wd, const u16* a_c, const u16* a_wmod, u16* a_out, unsigned char* a_ws) {
    __shared__ __attribute__((aligned(16))) unsigned char smem[163840];
    LAS unsigned char* L = (LAS unsigned char*)smem;
    typedef unsigned v16u __attribute__((ext_vector_type(16)));
    typedef unsigned v8u __attribute__((ext_vector_type(8)));
    v16u ka0; v8u ka1; v2u ka2; u64 pc0;
    asm volatile("s_load_dwordx16 %0, %4, 0x0\n\ts_load_dwordx8 %1, %4, 0x40\n\ts_load_dwordx2 %2, %4, 0x60\n\ts_getpc_b64 %3"
                 : "=&s"(ka0), "=&s"(ka1), "=&s"(ka2), "=&s"(pc0) : "s"(__builtin_amdgcn_kernarg_segment_ptr()));
#define KWAIT() do { SCHED(); asm volatile("s_waitcnt lgkmcnt(0)" : "+s"(ka0), "+s"(ka1), "+s"(ka2)); } while (0)
#define KPTR(v, i) ((u64)v[2 * (i)] | ((u64)v[2 * (i) + 1] << 32))
    const u64 t_start = rt();
    const int bid = blockIdx.x, tid = threadIdx.x, lane = tid & 63, w = __builtin_amdgcn_readfirstlane(tid >> 6);
    const int l15 = lane & 15, kq = lane >> 4, half = lane >> 5;
    const int a = bid & 127, xcd = bid & 7, jj = (bid >> 3) & 7;
    const int hh = 4 * xcd + (jj >> 1), cj = jj & 1;
    const int job0 = bid >= 128 ? 4 : (bid < 64 ? 2 : 0) + (w >> 2);
    int job = job0;
    unsigned ts[16] = {0, 0, 0, 0, 0, 0, 0, 0, 0, 0, 0, 0, 0, 0, 0, 0};
    ts[0] = (unsigned)t_start;
    unsigned tag0, sc[6]; v4u s1w[6];
    const int s1t = bid - 64, kp = (s1t * 57) >> 10, nt1 = s1t - 18 * kp;
    constexpr int UKL = 81920, HROW = 147456, QROW = HROW + 1152, SCR = 155648;
    asm volatile("" : "+s"(job));
    if (job == 0) {
        const int r1 = 192 * kp + 48 * w + (lane >> 3);
        int s0 = nt1 * 128 + r1 * 6912 + (lane & 7) * 16, s1 = r1 * 2;
        PIN(s0); PIN(s1);
        if (bid >= 120 && tid < CODE_BYTES / 128) {
            asm volatile("s_mov_b32 m0, %2\n\ts_nop 0\n\tglobal_load_lds_dword %0, %1" :: "v"(tid * 128), "s"(pc0 & ~4095ull), "i"(SCR) : "memory");
        }
        KWAIT();
        const rsrc_t rmod = RSRC(KPTR(ka1, 2), M * 6 * M * 2), rc = RSRC(KPTR(ka1, 1), M * 2);
#pragma unroll
        for (int i = 0; i < 6; ++i) asm volatile("buffer_load_ushort %0, %1, %2, 0 offen offset:%3" : "=v"(sc[i]) : "v"(s1), "s"(rc), "i"(16 * i) : "memory");
        asm volatile("global_load_dword %0, %1, %2 sc1" : "=v"(tag0) : "v"(WS_EPOCH), "s"(KPTR(ka2, 0)) : "memory");
#pragma unroll
        for (int i = 0; i < 6; ++i) asm volatile("buffer_load_dwordx4 %0, %1, %2, %3 offen nt" : "=v"(s1w[i]) : "v"(s0), "s"(rmod), "s"(8 * i * 6912) : "memory");
        SCHED();
        RAWBAR();
        STAMP(10);
    }
    int uko, uks, ukl;
#define UK_LOADS(lo, hi, step) do { const rsrc_t rk = RSRC(KPTR(ka0, 3), DC * 8192); \
        _Pragma("unroll 1") for (int i_ = (lo); i_ < (hi); ++i_) \
            dma16(rk, ukl + i_ * (step), uko ^ ((i_ & 3) << 6), uks + i_ * 32768); } while (0)
    asm volatile("" : "+s"(job));
    if (job == 3) {
        uko = kq * 8192 + ((l15 ^ kq) * 16); uks = (256 * cj + 64 * (w - 4)) * 8192 + hh * 256; ukl = UKL + (w - 4) * 16384;
        PIN(uko);
        KWAIT();
        UK_LOADS(0, 16, 1024);
        RAWBAR();
    }
    int kvo, kvs, kvl;
#define KV_LOADS(n, W) do { const rsrc_t rkv = RSRC(KPTR(ka0, 1), SK * DC * 2); \
        _Pragma("unroll 1") for (int i_ = 0; i_ < (n); ++i_) { \
            dma16(rkv, kvl, kvo, kvs); \
            kvo ^= 32; kvs = KVMAP ? ((kvs + 1048576) & ~0xFE000) | ((kvs + KVROT * 8192) & 0xFE000) : kvs + 8192; kvl += 8192; \
            asm volatile("s_waitcnt vmcnt(%0)" :: "i"((W) - 1) : "memory"); } } while (0)
    asm volatile("" : "+s"(job));
    if (job == 4 || job == 1) {
        int po = xcd * 1024 + lane * 16;
        PIN(po);
        KWAIT();
        if (job == 1) RAWBAR();
        const int n1 = job == 1 ? 896 : 736, ns = job == 1 ? 32 : 128;
#pragma unroll 1
        for (int n = job == 1 ? 704 + 8 * w + jj : 16 * w + (a >> 3); n < n1; n += ns) {
            const bool uk = n < 320;
            dma16(RSRC(uk ? KPTR(ka0, 3) : KPTR(ka0, 2), M * 8192), SCR + w * 1024, po, (uk ? 16 * (n & 28) + 24 + 4 * (n >> 5) + (n & 3) : n - 320) * 8192);
            if (job == 4) asm volatile("s_waitcnt vmcnt(%0)" :: "i"(PFW - 1) : "memory");
        }
        if (job == 1) RAWBAR();
    }
    asm volatile("" : "+s"(job));
    if (job == 2) {
        KWAIT();
        const unsigned e = ld32_sc1(RSRC(KPTR(ka2, 0), WS_BYTES), WS_EPOCH);
        *(LAS v4u*)(L + SCR + tid * 16) = __builtin_amdgcn_raw_buffer_load_b128(RSRC(KPTR(ka0, 0), M * 2), tid * 16, 0, 0);
        *(LAS unsigned*)(L + SCR + 4096) = e + 1u;
        LGKM0();
    }
    asm volatile("" : "+s"(job));
    if (job == 0) {
        const rsrc_t rws = RSRC(KPTR(ka2, 0), WS_BYTES);
        if (s1t >= 54 && s1t < 63) {
            const v4u poison = {POISON, POISON, POISON, POISON};
            const int u = (s1t - 54) * 4 + w;
#pragma unroll 1
            for (int i = 0; i < 8; ++i) __builtin_amdgcn_raw_buffer_store_b128(poison, rws, lane * 16, WS_QA + u * 8192 + i * 1024, AUX_SC1);
            asm volatile("s_waitcnt vmcnt(0)" : "+v"(tag0) :: "memory");
            __builtin_amdgcn_raw_buffer_store_b32(tag0 + 1u, rws, lane * 4, WS_CL + u * 256, AUX_SC1);
        }
        ZERO8(acc);
        int ra = (w * 64 + (lane & 7) * 8 + (lane >> 3)) * 4, rr = l15 * 16, so = (s1t < 54 && lane < 16) ? WS_H1 + kp * 4608 + (nt1 * 16 + lane) * 16 : OOB;
        PIN(ra); PIN(rr); PIN(so);
        SCHED();
        unsigned slo[6], shi[6];
        asm volatile("s_waitcnt vmcnt(6)" : "+v"(sc[0]), "+v"(sc[1]), "+v"(sc[2]), "+v"(sc[3]), "+v"(sc[4]), "+v"(sc[5]), "+v"(tag0));
        tag0 += 1u; PIN(tag0);
#pragma unroll
        for (int i = 0; i < 6; ++i) { shi[i] = f2u(silu_bf(bflo(sc[i]))); slo[i] = shi[i] >> 16; PIN(shi[i]); PIN(slo[i]); }
        SCHED();
#pragma unroll
        for (int i = 0; i < 6; ++i) { asm volatile("s_waitcnt vmcnt(%1)" : "+v"(s1w[i]) : "i"(5 - i)); dot8z(acc, s1w[i], slo[i], shi[i]); SCHED(); }
        *(LAS float*)(L + ra) = red8cols(acc, lane);
        BAR();
        if (w == 0) {
            LAS v4f* R = (LAS v4f*)(L + rr);
            const v4f s = (R[0] + R[16]) + (R[32] + R[48]);
            st_sc1(pack4(s.x, s.y, s.z, s.w, tag0), rws, so);
        }
    }
    const u64 x = KPTR(ka0, 0), ckv = KPTR(ka0, 1), wq = KPTR(ka0, 2), wuv = KPTR(ka0, 4), wo = KPTR(ka0, 5), wg = KPTR(ka0, 6), wu = KPTR(ka0, 7);
    const u64 wd = KPTR(ka1, 0), cc = KPTR(ka1, 1), wmod = KPTR(ka1, 2);
    GAS u16* out = (GAS u16*)KPTR(ka1, 3);
    GAS unsigned char* ws = (GAS unsigned char*)KPTR(ka2, 0);
    const rsrc_t rws = RSRC((u64)ws, WS_BYTES), rx = RSRC(x, M * 2), rmod = RSRC(wmod, M * 6 * M * 2), rc = RSRC(cc, M * 2);
    SCHED();
    STAMP(9);
    asm volatile("" : "+s"(job));

    if (job < 4) {
        const int b = bid;
        LAS float* red = (LAS float*)L;
        const int clo = WS_CL + (lane < 36 ? lane : 0) * 256;
        unsigned tag;
        const bool slowf = SLOW_EARLY && b >= 56 && ((((unsigned)(wg >> 7) + b - 56) & 7) == 3 || (((unsigned)(wu >> 7) + b - 56) & 7) == 3);
        if (job < 2) {
            RAWBAR();
            if (CH_DELAY) { while (rt() - t_start < (u64)CH_DELAY) __builtin_amdgcn_s_sleep(1); }
            const rsrc_t rkv = RSRC(ckv, SK * DC * 2);
#pragma unroll 1
            for (int i = slowf ? 32 : CH0 / 4; i < 32; ++i) {
                dma16(rkv, SCR + w * 1024, lane * 16, (i >> 1) * 1048576 + (((xcd + 8 * (2 * jj + (i & 1)) + KVROT * (i >> 1)) & 127) * 8 + w) * 1024);
                asm volatile("s_waitcnt vmcnt(%0)" :: "i"(CHW - 1) : "memory");
            }
            tag = ld32_sc1(rws, WS_EPOCH) + 1u;
            if (w == 0) {
#pragma unroll 1
                for (int sp = 0; sp < SPIN_MAX; ++sp) { MEMBAR(); if (__all(ld32_sc1(rws, clo) == tag)) break; }
            }
            DRAIN(); RAWBAR();
            STAMP(5);
        } else if (job == 2) {
            v4u bf[2][18];
            {
                const rsrc_t rq = RSRC(wq, M * 8192);
                const int qv = kq * 8192 + hh * 256 + ((l15 ^ (2 * kq) ^ ((w & 2) << 2)) * 16);
                const int q = l15 >> 2, p = lane & 3;
                int t0 = (8 * kq + q) * 256 + (((4 * w + (p >> 1)) ^ (2 * q) ^ ((kq & 1) << 3)) * 16) + 8 * (p & 1), t1 = t0 ^ 32;
                PIN(t0); PIN(t1);
#define WQ_LOADS(lo, hi) _Pragma("unroll 1") for (int i_ = (lo); i_ < (hi); ++i_) \
                    dma16(rq, (4 * i_ + w) * 1024, qv, (4 * i_ + w) * 32768)
#define WQ_FRAGS(lo, hi) _Pragma("unroll") for (int k = (lo); k < (hi); ++k) _Pragma("unroll") for (int m = 0; m < 2; ++m) { \
                    const int tb = (m ? t1 : t0) + (k / 6) * 49152; \
                    const v4s lo_ = __builtin_amdgcn_ds_read_tr16_b64_v4i16((LAS v4s*)(L + tb + (k % 6) * 8192)); \
                    const v4s hi_ = __builtin_amdgcn_ds_read_tr16_b64_v4i16((LAS v4s*)(L + tb + (k % 6) * 8192 + 1024)); \
                    bf[m][k] = __builtin_bit_cast(v4u, __builtin_shufflevector(lo_, hi_, 0, 1, 2, 3, 4, 5, 6, 7)); }
                RAWBAR();
                WQ_LOADS(0, 20);
                RAWBAR();
                WQ_LOADS(20, 36);
                asm volatile("s_waitcnt vmcnt(16)\n\ts_barrier" ::: "memory");
                WQ_FRAGS(0, 10);
                SCHED();
                asm volatile("s_waitcnt vmcnt(0)\n\ts_barrier" ::: "memory");
                STAMP(12);
                WQ_FRAGS(10, 18);
#pragma unroll
                for (int m = 0; m < 2; ++m)
#pragma unroll
                    for (int k = 0; k < 18; ++k) PIN(bf[m][k]);
            }
            tag = *(LAS unsigned*)(L + SCR + 4096); PIN(tag);
            int aa = HROW + kq * 16, qa = QROW + (32 * w + l15) * 2;
            PIN(aa); PIN(qa);
            STAMP(11);
            RAWBAR();
            STAMP(2);
            v4f a0 = {0.f, 0.f, 0.f, 0.f}, a1 = {0.f, 0.f, 0.f, 0.f};
            v4u av[18];
#pragma unroll
            for (int k = 0; k < 6; ++k) av[k] = *(LAS v4u*)(L + aa + k * 64);
            SCHED();
#pragma unroll
            for (int k = 0; k < 18; ++k) {
                a0 = mfma16(av[k], bf[0][k], a0); a1 = mfma16(av[k], bf[1][k], a1);
                if (k + 6 < 18) av[k + 6] = *(LAS v4u*)(L + aa + (k + 6) * 64);
                SCHED();
            }
            const unsigned qp = pkbf(a0.x, a1.x);
            *(LAS u16*)(L + qa) = (u16)qp; *(LAS u16*)(L + qa + 32) = (u16)(qp >> 16);
            BAR();
            STAMP(3);
            RAWBAR();
        } else {
            const int v = w - 4, j36 = lane < 36 ? lane : 35;
            DRAIN();
            STAMP(10);
            v4u uf[4][4];
#pragma unroll
            for (int t = 0; t < 4; ++t)
#pragma unroll
                for (int k = 0; k < 4; ++k) { uf[t][k] = *(LAS v4u*)(L + UKL + v * 16384 + (16 * t + l15) * 256 + (((4 * k + kq) ^ l15) * 16)); }
#pragma unroll
            for (int t = 0; t < 4; ++t)
#pragma unroll
                for (int k = 0; k < 4; ++k) PIN(uf[t][k]);
            BAR();
            float n0, n1, n2, n3;
            const v4u xa = *(LAS v4u*)(L + SCR + lane * 16);
            const unsigned xb = *(LAS unsigned*)(L + SCR + 1024 + lane * 4);
            const v2u xm = *(LAS v2u*)(L + SCR + (36 * v + j36) * 8);
            {
                const unsigned a0 = xa.x, a1 = xa.y, a2 = xa.z, a3 = xa.w, m0 = xm.x, m1 = xm.y;
                float vv[10] = {bflo(a0), bfhi(a0), bflo(a1), bfhi(a1), bflo(a2), bfhi(a2), bflo(a3), bfhi(a3), bflo(xb), bfhi(xb)};
                float s = 0.f;
#pragma unroll
                for (int i = 0; i < 10; ++i) s += vv[i];
                const float mean = wave_sum(s) * (1.f / M);
                float q = 0.f;
#pragma unroll
                for (int i = 0; i < 8; ++i) q += (vv[i] - mean) * (vv[i] - mean);
                if (lane < 32) q += (vv[8] - mean) * (vv[8] - mean) + (vv[9] - mean) * (vv[9] - mean);
                const float var = rb(wave_sum(q) * (1.f / M));
                const float den = rb(__builtin_amdgcn_sqrtf(rb(var + 1e-5f))), mb = rb(mean);
                n0 = rb(rb(bflo(m0) - mb) / den); n1 = rb(rb(bfhi(m0) - mb) / den); n2 = rb(rb(bflo(m1) - mb) / den); n3 = rb(rb(bfhi(m1) - mb) / den);
            }
            tag = *(LAS unsigned*)(L + SCR + 4096);
            int o = WS_H1 + (36 * v + j36) * 16, hw = HROW + (36 * v + j36) * 8, qr = QROW + kq * 16, fo = v == 0 ? clo : WS_CL;
            int qs = l15 < 4 ? WS_QA + hh * 1024 + (256 * cj + 64 * v + 16 * l15 + 4 * kq) * 2 : OOB;
            PIN(n0); PIN(n1); PIN(n2); PIN(n3); PIN(tag); PIN(o); PIN(hw); PIN(qr); PIN(qs); PIN(fo);
            STAMP(11);
            RAWBAR();
            v4u g[6]; unsigned cf;
#define H1_LOADS() do { _Pragma("unroll") for (int i = 0; i < 6; ++i) g[i] = __builtin_amdgcn_raw_buffer_load_b128(rws, o, 2304 * i, AUX_SC1); cf = ld32_sc1(rws, fo); } while (0)
#define H1_OK() __all(g[0].w == tag && g[1].w == tag && g[2].w == tag && g[3].w == tag && g[4].w == tag && g[5].w == tag && cf == tag)
            H1_LOADS();
            SCHED();
            RAWBAR();
            if (__builtin_expect(!H1_OK(), 0)) {
#pragma unroll 1
                for (int sp = 0; sp < SPIN_MAX; ++sp) { MEMBAR(); H1_LOADS(); if (H1_OK()) break; }
            }
            STAMP(12);
            {
                const v4f sa = unpack4n(g[0]) + unpack4n(g[2]) + unpack4n(g[4]), ca = unpack4n(g[1]) + unpack4n(g[3]) + unpack4n(g[5]);
                const unsigned s01 = pkbf(sa.x, sa.y), s23 = pkbf(sa.z, sa.w), c01 = pkbf(ca.x, ca.y), c23 = pkbf(ca.z, ca.w);
                const unsigned d01 = pkbf(1.f + bflo(c01), 1.f + bfhi(c01)), d23 = pkbf(1.f + bflo(c23), 1.f + bfhi(c23));
                const unsigned t01 = pkbf(n0 * bflo(d01), n1 * bfhi(d01)), t23 = pkbf(n2 * bflo(d23), n3 * bfhi(d23));
                *(LAS v2u*)(L + hw) = v2u{pkbf(bflo(t01) + bflo(s01), bfhi(t01) + bfhi(s01)), pkbf(bflo(t23) + bflo(s23), bfhi(t23) + bfhi(s23))};
            }
            BAR();
            RAWBAR();
            STAMP(4);
            {
                v4u qf[4];
#pragma unroll
                for (int k = 0; k < 4; ++k) qf[k] = *(LAS v4u*)(L + qr + k * 64);
                v4f y[4] = {};
#pragma unroll
                for (int t = 0; t < 4; ++t)
#pragma unroll
                    for (int k = 0; k < 4; ++k) y[t] = mfma16(uf[t][k], qf[k], y[t]);
                const v4f z = ((lane & 2) ? ((lane & 1) ? y[3] : y[2]) : ((lane & 1) ? y[1] : y[0])) * (0.08838834764831845f * 1.4426950408889634f);
                st64_sc1(v2u{pkbf(z.x, z.y), pkbf(z.z, z.w)}, rws, qs);
            }
            STAMP(5);
            RAWBAR();
        }
        if (!slowf) { const u64 dl = b >= 56 ? LATE_F : LATE_DELAY; while (rt() - t_start < dl) __builtin_amdgcn_s_sleep(1); }
        if (b >= 56) {
            const int f = b - 56;
            constexpr int FA = 147456, FS = FA + 4 * 1152, FG = FS + 64, FD = 155648;
            if (w < 4) {
                {
                    const rsrc_t rg = RSRC((w & 2) ? wu : wg, M * FF * 2);
                    const int r8 = lane >> 3, base = (288 * (w & 1) + r8) * 9216 + f * 128;
                    const int v0 = base + (((lane & 7) ^ (r8 & 2)) * 16), v1 = base + (((lane & 7) ^ ((r8 & 2) | 4)) * 16);
#pragma unroll
                    for (int i = 0; i < 36; ++i) {
                        dma16(rg, w * 36864 + i * 1024, (i & 1) ? v1 : v0, 8 * i * 9216);
                        if (i < FN0 && i >= FW0 - 1) asm volatile("s_waitcnt vmcnt(%0)" :: "i"(FW0 - 1) : "memory");
                        if (i == 30) RAWBAR();
                    }
                    const rsrc_t rd = RSRC(wd, M * FF * 2);
                    const int db = (64 * f + 16 * w + r8) * 1152 + 1024;
                    dma16(rd, FD + w * 2048, db + (((lane & 7) ^ (r8 & 2)) * 16), 0);
                    dma16(rd, FD + w * 2048 + 1024, db + (((lane & 7) ^ ((r8 & 2) | 4)) * 16), 8 * 1152);
                    DRAIN();
                }
                RAWBAR();
                STAMP(6);
                v4u bf[2][18], ef[2];
                {
                    const int q = l15 >> 2, p = lane & 3;
                    const int ta = (8 * kq + q) * 128 + (((2 * w + (p >> 1)) ^ ((q & 2) | ((kq & 1) << 2))) * 16) + 8 * (p & 1);
#pragma unroll
                    for (int m = 0; m < 2; ++m)
#pragma unroll
                        for (int k = 0; k < 18; ++k) {
                            int tb = ta + m * 73728 + (k >= 9 ? 36864 : 0);
                            const v4s lo = __builtin_amdgcn_ds_read_tr16_b64_v4i16((LAS v4s*)(L + tb + (k % 9) * 4096));
                            const v4s hi = __builtin_amdgcn_ds_read_tr16_b64_v4i16((LAS v4s*)(L + tb + (k % 9) * 4096 + 512));
                            bf[m][k] = __builtin_bit_cast(v4u, __builtin_shufflevector(lo, hi, 0, 1, 2, 3, 4, 5, 6, 7));
                        }
#pragma unroll
                    for (int k = 0; k < 2; ++k) {
                        const v4s lo = __builtin_amdgcn_ds_read_tr16_b64_v4i16((LAS v4s*)(L + (ta + FD) + k * 4096));
                        const v4s hi = __builtin_amdgcn_ds_read_tr16_b64_v4i16((LAS v4s*)(L + (ta + FD) + k * 4096 + 512));
                        ef[k] = __builtin_bit_cast(v4u, __builtin_shufflevector(lo, hi, 0, 1, 2, 3, 4, 5, 6, 7)); PIN(ef[k]);
                    }
#pragma unroll
                    for (int m = 0; m < 2; ++m)
#pragma unroll
                        for (int k = 0; k < 18; ++k) PIN(bf[m][k]);
                }
                int aa = FA + (l15 == 0 ? 0 : (l15 == 4 ? 1152 : (l15 == 8 ? 2304 : 3456))) + kq * 16, ga = FG + (16 * w + l15) * 2;
                int gra = FG + kq * 16, ho = l15 == 0 ? WS_H6 + f * 2304 + 2048 + w * 64 + kq * 16 : OOB;
                PIN(aa); PIN(ga); PIN(gra); PIN(ho);
                STAMP(13);
                RAWBAR();
                STAMP(7);
                v4f ag = {0.f, 0.f, 0.f, 0.f}, au = {0.f, 0.f, 0.f, 0.f};
                v4u av[18];
#pragma unroll
                for (int k = 0; k < 6; ++k) av[k] = *(LAS v4u*)(L + aa + k * 64);
                SCHED();
#pragma unroll
                for (int k = 0; k < 18; ++k) {
                    ag = mfma16(av[k], bf[0][k], ag); au = mfma16(av[k], bf[1][k], au);
                    if (k + 6 < 18) av[k + 6] = *(LAS v4u*)(L + aa + (k + 6) * 64);
                    if (k == 5) RAWBAR();
                    if (k == 13) RAWBAR();
                    SCHED();
                }
                const v2f st = *(LAS v2f*)(L + FS + 32);
                const float cf = kq == 0 ? st.x : (kq == 1 ? -st.y : 1.f);
                float z = red32(ag.x * cf, au.x * cf); z = red16(z, z);
                auto sw = __builtin_amdgcn_permlane32_swap(f2u(z), f2u(z), false, false);
                const unsigned zg = sw[0], zu = sw[1];
                const unsigned gu = pkbf(u2f(zg), u2f(zu));
                const float a1 = bflo(gu), a2 = bfhi(gu);
                constexpr float K0 = -2.f * 1.4426950408889634f * 0.7978845608028654f, K1 = K0 * 0.044715f;
                const float e = __builtin_amdgcn_exp2f(a1 * (K0 + K1 * a1 * a1));
                const float ge = rb(a1 * __builtin_amdgcn_rcpf(1.f + e));
                *(LAS u16*)(L + ga) = (u16)pkbf(ge * a2, 0.f);
                BAR();
                STAMP(14);
                const v4u g0 = *(LAS v4u*)(L + gra), g1 = *(LAS v4u*)(L + gra + 64);
                const v4f y0 = {0.f, 0.f, 0.f, 0.f};
                st_sc1(__builtin_bit_cast(v4u, mfma16(ef[1], g1, mfma16(ef[0], g0, y0))), rws, ho);
                STAMP(8);
            } else {
                const int v = w - 4;
                v4u dr[2][8];
                {
                    const rsrc_t rd = RSRC(wd, M * FF * 2);
                    const int dofs = (64 * f + 8 * kq) * 1152 + v * 256 + l15 * 16;
                    RAWBAR();
#pragma unroll
                    for (int u = 0; u < 2; ++u)
#pragma unroll
                        for (int e = 0; e < 8; ++e) dr[u][e] = __builtin_amdgcn_raw_buffer_load_b128(rd, dofs, (32 * u + e) * 1152, AUXL);
                    SCHED();
                    RAWBAR();
                }
                float xv[3], gav[3], c1v[3]; int to[3], ua[3];
                {
                    unsigned xr[3], gr[3], cr[3]; v4u c4, s4;
                    const int g16 = tid - 256;
                    const int co = g16 < 72 ? WS_MRB + 2304 + g16 * 16 : (g16 < 144 ? WS_MRB + 1152 + (g16 - 72) * 16 : OOB);
#pragma unroll
                    for (int t = 0; t < 3; ++t) {
                        const bool on = t < 2 || v == 3;
                        const int cg = 16 * (2 * v + t) + l15, m = 4 * cg + 2 * half + (kq & 1);
                        to[t] = on ? WS_T5 + kq * 2304 + cg * 16 : OOB; ua[t] = on ? FA + m * 2 : FG + 128 + lane * 2;
                        xr[t] = __builtin_amdgcn_raw_buffer_load_b16(rx, on ? m * 2 : OOB, 0, 0);
                    }
#pragma unroll 1
                    for (int sp = 0;; ++sp) {
                        MEMBAR();
                        unsigned mx = 0;
#pragma unroll
                        for (int t = 0; t < 3; ++t) {
                            const int mo = (t < 2 || v == 3) ? WS_MRB + ((16 * (2 * v + t) + l15) * 4 + 2 * half) * 2 : OOB;
                            gr[t] = ld32_sc1(rws, mo); cr[t] = ld32_sc1(rws, mo + 2304); mx = umax(umax(mx, gr[t]), cr[t]);
                        }
                        c4 = ld_sc1(rws, co);
                        if (__all(umax(umax(mx, c4.x), c4.z) != POISON) || sp > SPIN_MAX) break;
                        __builtin_amdgcn_s_sleep(8);
                    }
                    if (g16 < 216) *(LAS v4u*)(L + FA + 1152 + g16 * 16) = c4;
#pragma unroll
                    for (int t = 0; t < 3; ++t) {
                        xv[t] = bflo(xr[t]); gav[t] = (kq & 1) ? bfhi(gr[t]) : bflo(gr[t]); c1v[t] = (kq & 1) ? bfhi(cr[t]) : bflo(cr[t]);
                        PIN(xv[t]); PIN(gav[t]); PIN(c1v[t]); PIN(to[t]); PIN(ua[t]);
                    }
                }
                STAMP(6);
                int sa = FS + half * 16 + v * 4, gra = FG + kq * 16, ho = lane < 32 ? WS_H6 + f * 2304 + v * 512 + l15 * 32 + kq * 16 : OOB;
                PIN(sa); PIN(gra); PIN(ho);
                v4u df[2][8];
#pragma unroll
                for (int u = 0; u < 2; ++u) {
#pragma unroll
                    for (int p = 0; p < 4; ++p) interleave(dr[u][2 * p], dr[u][2 * p + 1]);
#pragma unroll
                    for (int n = 0; n < 8; ++n) { df[u][n] = v4u{dr[u][n >> 2][n & 3], dr[u][2 + (n >> 2)][n & 3], dr[u][4 + (n >> 2)][n & 3], dr[u][6 + (n >> 2)][n & 3]}; PIN(df[u][n]); }
                }
                STAMP(13);
                cue<false>(rws, WS_CTX + f * 512 + v * 128, 0, CUE7);
                float x2[3];
#pragma unroll 1
                for (int sp = 0;; ++sp) {
                    MEMBAR();
                    v4u t4[3];
#pragma unroll
                    for (int t = 0; t < 3; ++t) t4[t] = ld_sc1(rws, to[t]);
#pragma unroll
                    for (int t = 0; t < 3; ++t) { const unsigned t0 = t4[t].x, t1 = t4[t].y, t2 = t4[t].z, t3 = t4[t].w; x2[t] = red16(red32(u2f(t0), u2f(t2)), red32(u2f(t1), u2f(t3))); }
                    const float chk = x2[0] + x2[1] + x2[2];
                    if (__all(chk == chk) || sp > SPIN_MAX) break;
                }
                STAMP(7);
#pragma unroll
                for (int t = 0; t < 3; ++t) {
                    x2[t] = rb(xv[t] + rb(gav[t] * rb(x2[t])));
                    *(LAS u16*)(L + ua[t]) = (u16)pkbf(x2[t] * c1v[t], 0.f);
                }
                BAR();
                float s = red32(x2[0] + x2[1] + x2[2], x2[0] * x2[0] + x2[1] * x2[1] + x2[2] * x2[2]); s = row_sum(red16(s, s));
                *(LAS float*)(L + sa) = s;
                BAR();
                if (v == 0) {
                    const float mean = sum4(*(LAS v4f*)(L + FS)) * (1.f / M);
                    const float var = rb(fmaxf(sum4(*(LAS v4f*)(L + FS + 16)) * (1.f / M) - mean * mean, 0.f));
                    const float rs = __builtin_amdgcn_rcpf(rb(__builtin_amdgcn_sqrtf(rb(var + 1e-5f))));
                    *(LAS v2f*)(L + FS + 32) = v2f{rs, rb(mean) * rs};
                }
                BAR();
                RAWBAR();
                STAMP(14);
                const v4u g0 = *(LAS v4u*)(L + gra), g1 = *(LAS v4u*)(L + gra + 64);
                v4f y[8] = {};
#pragma unroll
                for (int n = 0; n < 8; ++n) y[n] = mfma16(g1, df[1][n], mfma16(g0, df[0][n], y[n]));
                const bool up = kq & 1;
                st_sc1(v4u{f2u(up ? y[4].x : y[0].x), f2u(up ? y[5].x : y[1].x), f2u(up ? y[6].x : y[2].x), f2u(up ? y[7].x : y[3].x)}, rws, ho);
                STAMP(8);
            }
        } else if (b >= 20) {
            const int o6 = b - 20, kg = o6 / 9, nt = o6 - 9 * kg;
            const int head = 8 * kg + w;
            v4u of[4][4];
            {
                const rsrc_t ro = RSRC(wo, 4096 * M * 2);
                const int oo = (128 * head + 8 * kq) * 1152 + nt * 128 + l15 * 8;
                v2u orr[4][8];
#pragma unroll
                for (int u = 0; u < 4; ++u)
#pragma unroll
                    for (int e = 0; e < 8; ++e) orr[u][e] = ld64_nt(ro, oo, (32 * u + e) * 1152);
                SCHED();
#pragma unroll
                for (int u = 0; u < 4; ++u) frag4(orr[u], of[u]);
            }
            STAMP(6);
            STAMP(13);
            if (w == 0) cue<true>(rws, WS_ST + (3 * o6) * 256 + 12, __builtin_amdgcn_readfirstlane(tag), CUE6);
            RAWBAR();
            const int co = (l15 < 8 ? WS_CTX + ((head >> 1) * 8 + l15) * 512 + (head & 1) * 256 : OOB) + kq * 16;
            v4u a[4];
#pragma unroll 1
            for (int sp = 0;; ++sp) {
                MEMBAR();
#pragma unroll
                for (int u = 0; u < 4; ++u) a[u] = ld_sc1(rws, co + 64 * u);
                unsigned mx = 0;
#pragma unroll
                for (int u = 0; u < 4; ++u) mx = umax(umax(mx, a[u].x), a[u].z);
                if (__all(mx != POISON) || sp > SPIN_MAX) break;
            }
            STAMP(7);
            v4f acc[4] = {};
#pragma unroll
            for (int u = 0; u < 4; ++u)
#pragma unroll
                for (int n = 0; n < 4; ++n) acc[n] = mfma16(a[u], of[u][n], acc[n]);
            v2f c; c.x = red16(sum4(acc[0]), sum4(acc[2])); c.y = red16(sum4(acc[1]), sum4(acc[3]));
            if (lane < 32) *(LAS v2f*)(red + w * 64 + l15 * 4 + kq * 2) = c;
            BAR();
            if (tid < 32) {
                LAS v2f* R = (LAS v2f*)red + lane;
                const v2f s = ((R[0] + R[32]) + (R[64] + R[96])) + ((R[128] + R[160]) + (R[192] + R[224]));
                st64_sc1(v2u{f2u(s.x), f2u(s.y)}, rws, WS_T5 + kg * 2304 + nt * 256 + lane * 8);
            }
            STAMP(8);
        } else if (b < 18) {
            {
                v4u mw[18]; unsigned mc[18];
#pragma unroll
                for (int i = 0; i < 18; ++i) mc[i] = __builtin_amdgcn_raw_buffer_load_b16(rc, (72 * w + 4 * i + kq) * 2, 0, 0);
#pragma unroll
                for (int i = 0; i < 18; ++i) mw[i] = ld_nt(rmod, (72 * w + 4 * i + kq) * 6912 + 2304 + b * 256 + l15 * 16);
                SCHED();
#pragma unroll
                for (int i = 0; i < 18; ++i) { mc[i] = f2u(silu_bf(bflo(mc[i]))); PIN(mc[i]); }
                ZERO8(acc);
#pragma unroll
                for (int i = 0; i < 18; ++i) dot8z(acc, mw[i], mc[i] >> 16, mc[i]);
                const float b0 = red32(acc[0], acc[4]), b1 = red32(acc[1], acc[5]), b2 = red32(acc[2], acc[6]), b3 = red32(acc[3], acc[7]);
                v2f c; c.x = red16(b0, b2); c.y = red16(b1, b3);
                *(LAS v2f*)(red + w * 128 + l15 * 8 + kq * 2) = c;
                BAR();
                if (tid < 32) {
                    v4f s = *(LAS v4f*)(red + tid * 4);
#pragma unroll
                    for (int k = 1; k < 8; ++k) s += *(LAS v4f*)(red + k * 128 + tid * 4);
                    const int col = 128 * b + 4 * tid;
                    const float one = (col >= 1152 && col < 1728) ? 1.f : 0.f;
                    st64_sc1(v2u{pkbf(rb(s.x) + one, rb(s.y) + one), pkbf(rb(s.z) + one, rb(s.w) + one)}, rws, WS_MRB + col * 2);
                }
                STAMP(6);
            }
            if (w == 0) {
                const int cg = 8 * b + (lane & 7);
                const int m = 4 * cg + 2 * half + (kq & 1);
                v4u t; unsigned gb, gaw;
                const unsigned xr = __builtin_amdgcn_raw_buffer_load_b16(rx, m * 2, 0, 0);
                cue<false>(rws, WS_CTX + (72 + b) * 512, 0, CUE7);
#pragma unroll 1
                for (int sp = 0;; ++sp) {
                    MEMBAR();
                    gb = ld32_sc1(rws, WS_MRB + 3 * 1152 + (m & ~1) * 2); gaw = ld32_sc1(rws, WS_MRB + (m & ~1) * 2);
                    t = ld_sc1(rws, WS_T5 + kq * 2304 + cg * 16);
                    if (__all(umax(umax(t.x, t.z), umax(gb, gaw)) != POISON) || sp > SPIN_MAX) break;
                    __builtin_amdgcn_s_sleep(1);
                }
                const unsigned t0 = t.x, t1 = t.y, t2 = t.z, t3 = t.w;
                float x2 = rb(bflo(xr) + rb(((kq & 1) ? bfhi(gaw) : bflo(gaw)) * rb(red16(red32(u2f(t0), u2f(t2)), red32(u2f(t1), u2f(t3))))));
                float gsel = (kq & 1) ? bfhi(gb) : bflo(gb);
                PIN(x2); PIN(gsel);
                STAMP(7);
                { const u64 t0 = rt(); while (rt() - t0 < (u64)CUE8) __builtin_amdgcn_s_sleep(1); }
                v4f s;
#pragma unroll 1
                for (int sp = 0;; ++sp) {
                    MEMBAR();
                    v4u y[9];
#pragma unroll
                    for (int i = 0; i < 9; ++i) y[i] = ld_sc1(rws, WS_H6 + (8 * i + (lane >> 3)) * 2304 + cg * 16);
                    SCHED();
                    s = __builtin_bit_cast(v4f, y[0]);
#pragma unroll
                    for (int i = 1; i < 9; ++i) s += __builtin_bit_cast(v4f, y[i]);
                    if (__all(s.x == s.x) || sp > SPIN_MAX) break;
                }
                const float yv = rb(add_x8(red16(red32(s.x, s.z), red32(s.y, s.w))));
                const float o = x2 + rb(gsel * yv);
                if ((lane & 8) == 0) out[m] = (u16)pkbf(o, o);
                if (b == 0 && lane == 0) st32_sc1(tag, rws, WS_EPOCH);
                STAMP(8);
            }
        }
    } else {
        unsigned f1, tag;
        int f1o = WS_CL + (lane & 3) * 256; PIN(f1o);
        kvo = (lane ^ (((w & 3) << 2) | ((w >> 2) & 3))) * 16; kvs = (KVMAP == 0 ? a * 128 + w : (KVMAP == 1 ? w * 128 + a : a * 8 + w)) * 1024; kvl = w * 1024;
        if (A_DELAY) { while (rt() - t_start < (u64)A_DELAY) {} }
        STAMP(1);
        KV_LOADS(DMAN0, DMAW0);
        KV_LOADS(12 - DMAN0, DMAW);
        asm volatile("global_load_dword %0, %2, %4 sc1\n\tglobal_load_dword %1, %3, %4 sc1" : "=&v"(f1), "=&v"(tag) : "v"(f1o), "v"(WS_EPOCH), "s"(ws) : "memory");
        KV_LOADS(4, DMAW);
        STAMP(2);
        const int hpair = a >> 3, cq = a & 7;
        unsigned ad[8];
        if (w < 4) {
            const int r = lane & 31, g = ((r & 3) << 2) | ((r >> 2) & 3);
#pragma unroll
            for (int j = 0; j < 8; ++j) { ad[j] = w * 32768 + r * 1024 + (((2 * j + half) ^ g) * 16); PIN(ad[j]); }
        } else {
            const int blk = kq & 1, q = l15 >> 2, p = lane & 3;
#pragma unroll
            for (int j = 0; j < 8; ++j) {
                const int t = j >> 1, tt = j & 1, g = (q << 2) | ((2 * tt + half) & 3);
                ad[j] = (8 * tt + 4 * half + q) * 1024 + (w - 4) * 256 + (((4 * t + 2 * blk + (p >> 1)) ^ g) * 16) + 8 * (p & 1); PIN(ad[j]);
            }
        }
        unsigned qw[4];
#pragma unroll
        for (int i = 0; i < 4; ++i) { qw[i] = QOFF + (4 * w + i) * 1024 + ((lane ^ ((i << 2) | (w & 3))) * 16); PIN(qw[i]); }
        int qo = WS_QA + 4 * w * 1024 + lane * 16; PIN(qo);
        asm volatile("s_waitcnt vmcnt(%2)" : "+v"(f1), "+v"(tag) : "i"(QWAIT) : "memory");
        tag += 1u;
        if (__builtin_expect(!__all(f1 == tag), 0)) {
#pragma unroll 1
            for (int sp = 0; sp < SPIN_MAX; ++sp) { MEMBAR(); if (__all(ld32_sc1(rws, f1o) == tag)) break; }
        }
        STAMP(11);
        v4u q[4];
#pragma unroll
        for (int i = 0; i < 4; ++i) q[i] = ld_sc1(rws, qo + i * 1024);
        SCHED();
#define Q_OK() __all(umax(umax(umax(q[0].x, q[0].z), umax(q[1].x, q[1].z)), umax(umax(q[2].x, q[2].z), umax(q[3].x, q[3].z))) != POISON)
#define Q_TO_LDS() do { \
            asm volatile("" : "+v"(q[0]), "+v"(q[1]), "+v"(q[2]), "+v"(q[3])); \
            if (__builtin_expect(!Q_OK(), 0)) { \
                _Pragma("unroll 1") for (int sp = 0; sp < SPIN_MAX; ++sp) { \
                    MEMBAR(); \
                    v4u qn[4]; \
                    _Pragma("unroll") for (int i = 0; i < 4; ++i) qn[i] = ld_sc1(rws, umax(q[i].x, q[i].z) == POISON ? qo + i * 1024 : OOB); \
                    _Pragma("unroll") for (int i = 0; i < 4; ++i) q[i] = umax(q[i].x, q[i].z) == POISON ? qn[i] : q[i]; \
                    if (Q_OK()) break; \
                } \
            } \
            STAMP(12); \
            _Pragma("unroll") for (int i = 0; i < 4; ++i) *(LAS v4u*)(L + qw[i]) = q[i]; \
        } while (0)
        constexpr int OST = QOFF + 16384, OFL = QOFF + 15360;
        RAWBAR();
        if (w < 4) {
            v4u kf[32], qf[32];
            if (w < 3) {
#pragma unroll
                for (int s = 0; s < 32; ++s) kf[s] = *(LAS v4u*)(L + ad[s & 7] + (s >> 3) * 256);
            }
            int mxr = QOFF + 8192 + (lane & 31) * 16, mxw = mxr + w * 4, pwa = QOFF + 2 * w * 1024 + lane * 16;
            PIN(mxr); PIN(mxw); PIN(pwa);
            unsigned aq[8];
#pragma unroll
            for (int j = 0; j < 8; ++j) { aq[j] = ad[j] + (QOFF - w * 32768); PIN(aq[j]); }
            Q_TO_LDS();
            BAR();
            STAMP(3);
            v16f acc = {};
            if (w < 3) {
                constexpr int PD = 8;
#pragma unroll
                for (int s = 0; s < PD; ++s) qf[s] = *(LAS v4u*)(L + aq[s & 7] + (s >> 3) * 256);
                SCHED();
#pragma unroll
                for (int s = 0; s < 32; ++s) {
                    acc = mfma32(kf[s], qf[s], acc);
                    if (s + PD < 32) qf[s + PD] = *(LAS v4u*)(L + aq[(s + PD) & 7] + ((s + PD) >> 3) * 256);
                    SCHED();
                }
            } else {
                constexpr int PD = 6;
#pragma unroll
                for (int s = 0; s < PD; ++s) { kf[s] = *(LAS v4u*)(L + ad[s & 7] + (s >> 3) * 256); qf[s] = *(LAS v4u*)(L + aq[s & 7] + (s >> 3) * 256); }
                SCHED();
#pragma unroll
                for (int s = 0; s < 32; ++s) {
                    acc = mfma32(kf[s], qf[s], acc);
                    if (s + PD < 32) { kf[s + PD] = *(LAS v4u*)(L + ad[(s + PD) & 7] + ((s + PD) >> 3) * 256); qf[s + PD] = *(LAS v4u*)(L + aq[(s + PD) & 7] + ((s + PD) >> 3) * 256); }
                    SCHED();
                }
            }
            RAWBAR();
            STAMP(13);
            float m = -3.0e38f;
#pragma unroll
            for (int i = 0; i < 16; ++i) m = fmaxf(m, acc[i]);
            {
                auto sw = __builtin_amdgcn_permlane32_swap(f2u(m), f2u(m), false, false); const unsigned x0 = sw[0], x1 = sw[1];
                m = __builtin_ceilf(fmaxf(fmaxf(m, u2f(x0)), u2f(x1)));
            }
            *(LAS float*)(L + mxw) = m;
            float p[16];
#pragma unroll
            for (int i = 0; i < 8; ++i) p[i] = __builtin_amdgcn_exp2f(acc[i] - m);
            SCHED();
            BAR();
            const v4f mk = *(LAS v4f*)(L + mxr);
            SCHED();
#pragma unroll
            for (int i = 8; i < 16; ++i) p[i] = __builtin_amdgcn_exp2f(acc[i] - m);
            unsigned pw[8];
#pragma unroll
            for (int i = 0; i < 8; ++i) pw[i] = pkbf(p[2 * i], p[2 * i + 1]);
            SCHED();
            const float mm = fmaxf(fmaxf(fmaxf(fmaxf(m, mk.x), mk.y), mk.z), mk.w);
            const float dm = fminf(mm - m, 255.f);
            const unsigned sub = (unsigned)(dm * 8388736.f);
#pragma unroll
            for (int i = 0; i < 8; ++i) pw[i] = __builtin_bit_cast(unsigned, __builtin_elementwise_sub_sat(__builtin_bit_cast(v2us, pw[i]), __builtin_bit_cast(v2us, sub)));
            *(LAS v4u*)(L + pwa) = v4u{pw[0], pw[1], pw[2], pw[3]};
            *(LAS v4u*)(L + pwa + 1024) = v4u{pw[4], pw[5], pw[6], pw[7]};
            SCHED();
            BAR();
            SCHED();
            STAMP(4);
            float l = 0.f;
#pragma unroll
            for (int i = 0; i < 16; ++i) l += p[i];
            l = red32(l, l) * __builtin_amdgcn_exp2f(-dm);
            *(LAS float*)(L + mxw + 512) = l;
            BAR();
            if (w == 1) {
                const float ls = sum4(*(LAS v4f*)(L + mxr + 512));
                st_sc1(pack4(mm, dpp<0xB1>(mm), ls, dpp<0xB1>(ls), tag), rws, (lane & 33) ? OOB : WS_ST + a * 256 + lane * 8);
            }
            int io = WS_INB + a * 32768 + (32 * w + 8 * kq) * 256 + l15 * 16, sto = WS_ST + (32 * w + (lane & 31)) * 256 + hpair * 16;
            int wtw = QOFF + 12288 + w * 128 + lane * 2, wtr = QOFF + 12288 + w * 128 + (lane & 1) * 64 + kq * 16, mw = QOFF + 12800 + half * 16;
            int cko = lane < 32 ? WS_CK + ((32 * w + lane) * 8 + cq) * 128 + hpair * 8 : OOB;
            int law = QOFF + 13312 + (kq & 1) * 512 + w * 128 + l15 * 8;
            {
                int r0 = OST + w * 2048 + lane * 16, r1 = kq * 1024 + w * 256 + l15 * 16, so = WS_INB + ((lane >> 3) * 8 + 2 * w) * 32768 + a * 256 + (lane & 7) * 16;
                int co = (lane & 6) ? OOB : WS_CK + (a * 8 + 2 * w) * 128 + ((lane >> 3) + 8 * (lane & 1)) * 8, fl = OFL + w * 4;
                PIN(r0); PIN(r1); PIN(so); PIN(co); PIN(fl);
                unsigned c0 = 0, c1 = 0;
#pragma unroll
                for (int t = 0; t < 4; ++t) {
                    if (t < 3) {
#pragma unroll 1
                        for (int sp = 0; sp < (1 << 22); ++sp) { MEMBAR(); if (__all(*(volatile LAS unsigned*)(L + fl + t * 16) == tag)) break; }
                        MEMBAR();
                    } else RAWBAR();
                    const v4u o0 = *(LAS v4u*)(L + (t ? r1 + t * 8192 : r0)), o1 = *(LAS v4u*)(L + (t ? r1 + t * 8192 + 4096 : r0 + 1024));
                    __builtin_amdgcn_raw_buffer_store_b128(o0, rws, so + (t & 1) * 128, (t >> 1) * 32768, AUX_SC1);
                    __builtin_amdgcn_raw_buffer_store_b128(o1, rws, so + (t & 1) * 128, (64 + (t >> 1)) * 32768, AUX_SC1);
                    SCHED();
                    c0 += (o0.x + o0.y) + (o0.z + o0.w); c1 += (o1.x + o1.y) + (o1.z + o1.w);
                    if (t & 1) {
                        c0 = addu_dpp<0xB1>(c0); c1 = addu_dpp<0xB1>(c1);
                        unsigned c = addu_dpp<0x4E>((lane & 1) ? c1 : c0);
                        c += (unsigned)__builtin_amdgcn_update_dpp(0, __builtin_amdgcn_update_dpp(0, (int)c, 0x1B, 0xf, 0xf, true), 0x141, 0xf, 0xf, true);
                        st64_sc1(v2u{c, tag}, rws, co + (t >> 1) * 128);
                        c0 = 0; c1 = 0;
                    } else { PIN(c0); PIN(c1); }
                    if (t == 0) { PIN(io); PIN(sto); PIN(wtw); PIN(wtr); PIN(mw); PIN(cko); PIN(law); }
                    SCHED();
                }
                STAMP(5);
            }
            v4u s0 = ld_sc1(rws, sto), d[8];
            PIN(s0);
            if (__builtin_expect(!__all(s0.w == tag), 0)) {
#pragma unroll 1
                for (int sp = 0; sp < SPIN_MAX; ++sp) { MEMBAR(); s0 = ld_sc1(rws, sto); if (__all(s0.w == tag)) break; }
            }
            STAMP(6);
            if (CUE5) { const u64 t0 = rt(); while (rt() - t0 < (u64)CUE5) {} }
#pragma unroll
            for (int e = 0; e < 8; ++e) { d[e] = ld_sc1(rws, io + e * 256); SCHED(); }
            v2u ck = __builtin_amdgcn_raw_buffer_load_b64(rws, cko, 0, AUX_SC1);
            SCHED();
            v4u wa;
            {
                const v4f sv = unpack4(s0);
                const float ms = half ? sv.y : sv.x, lv = half ? sv.w : sv.z;
                *(LAS float*)(L + mw + w * 4) = row_max(max16(ms));
                BAR();
                const v4f mk2 = *(LAS v4f*)(L + mw);
                const float e = rb(__builtin_amdgcn_exp2f(ms - fmaxf(fmaxf(fmaxf(fmaxf(ms, mk2.x), mk2.y), mk2.z), mk2.w)));
                *(LAS u16*)(L + wtw) = (u16)(f2u(e) >> 16);
                float ll = e * lv; ll = row_sum(red16(ll, ll));
                *(LAS float*)(L + mw + 32 + w * 4) = ll;
                LGKM0();
                wa = *(LAS v4u*)(L + wtr);
                PIN(wa);
            }
            STAMP(14);
            v2u lo2;
#define COMBINE(ok) do { \
                v4u lo[4], hi[4]; unsigned sm = 0; \
                _Pragma("unroll") for (int p2 = 0; p2 < 4; ++p2) { \
                    _Pragma("unroll") for (int t = 0; t < 4; ++t) { const unsigned u0 = d[2 * p2][t], u1 = d[2 * p2 + 1][t]; lo[t][p2] = perm(u1, u0, 0x05040100u); hi[t][p2] = perm(u1, u0, 0x07060302u); sm += u0 + u1; } \
                    SCHED(); } \
                const v4f z = {0.f, 0.f, 0.f, 0.f}; float la0[4], la1[4]; \
                _Pragma("unroll") for (int t = 0; t < 4; ++t) { la0[t] = mfma16(wa, lo[t], z).x; la1[t] = mfma16(wa, hi[t], z).y; } \
                sm -= ck.x; \
                sm = addu_dpp<0x124>(addu_dpp<0x128>(addu_dpp<0x4E>(addu_dpp<0xB1>(sm)))); \
                sm = redu16(sm, sm); sm = redu32(sm, sm); \
                ok = __all(sm == 0u && (ck.y == tag || lane >= 32)); \
                const unsigned a0 = pkbf(la0[0], la0[1]), a1 = pkbf(la0[2], la0[3]), b0 = pkbf(la1[0], la1[1]), b1 = pkbf(la1[2], la1[3]); \
                lo2 = (kq & 1) ? v2u{b0, b1} : v2u{a0, a1}; \
            } while (0)
            bool ok;
            COMBINE(ok);
            if (__builtin_expect(!ok, 0)) {
#pragma unroll 1
                for (int sp = 0; sp < SPIN_MAX && !ok; ++sp) {
#pragma unroll 1
                    for (int s2 = 0; s2 < SPIN_MAX && !__all(ck.y == tag || lane >= 32); ++s2) { MEMBAR(); ck = __builtin_amdgcn_raw_buffer_load_b64(rws, cko, 0, AUX_SC1); }
                    MEMBAR();
#pragma unroll
                    for (int e = 0; e < 8; ++e) d[e] = ld_sc1(rws, io + e * 256);
                    COMBINE(ok);
                    ts[15] += 1000;
                }
            }
            *(LAS v2u*)(L + law) = lo2;
            STAMP(7);
            BAR();
        } else {
            const int v = w - 4;
            const int col = lane & 31;
            int ob0 = OST + v * 2048 + 2 * half * 128 + col * 4, ob1 = half * 1024 + v * 256 + col * 4, fl = OFL + v * 4, pra = QOFF + lane * 16;
            const rsrc_t rv = RSRC(wuv, DC * 8192);
            int vo = (64 * cq + 8 * kq) * 8192 + hpair * 512 + v * 128 + l15 * 8;
            unsigned ah[8];
#pragma unroll
            for (int j = 0; j < 8; ++j) { ah[j] = ad[j] + 65536; PIN(ah[j]); }
            PIN(ob0); PIN(ob1); PIN(fl); PIN(pra); PIN(vo);
            Q_TO_LDS();
            BAR();
            v2u vr[2][8];
#pragma unroll
            for (int i = 0; i < UVA; ++i) vr[i >> 3][i & 7] = ld64_nt(rv, vo, (32 * (i >> 3) + (i & 7)) * 8192);
            SCHED();
            v4s vb[4][16];
#define VRD(t, k) vb[t][k] = __builtin_amdgcn_ds_read_tr16_b64_v4i16((LAS v4s*)(L + (((k) >> 3) ? ah[2 * (t) + ((k) & 1)] : ad[2 * (t) + ((k) & 1)]) + (((k) >> 1) & 3) * 16384))
#define VREAD(t) _Pragma("unroll") for (int k = 0; k < 16; ++k) VRD(t, k)
            RAWBAR();
            VREAD(0);
            SCHED();
            RAWBAR();
            VREAD(1);
            __builtin_amdgcn_s_setprio(2);
            BAR();
            STAMP(4); CYC0();
            v4u pa[8];
#pragma unroll
            for (int ks = 0; ks < 8; ++ks) pa[ks] = *(LAS v4u*)(L + pra + ks * 1024);
            SCHED();
            v16f acc[4];
#define OSTORE(t, e) (*(LAS unsigned*)(L + ((t) ? ob1 + (t) * 8192 + ((e) >> 1) * 2048 + ((e) & 1) * 128 : ob0 + (4 * ((e) >> 1) + ((e) & 1)) * 128)) = pkbf(acc[t][2 * (e)], acc[t][2 * (e) + 1]))
#pragma unroll
            for (int t = 0; t < 4; ++t) {
#pragma unroll
                for (int ks = 0; ks < 8; ++ks) {
                    const v4u bv = __builtin_bit_cast(v4u, __builtin_shufflevector(vb[t][2 * ks], vb[t][2 * ks + 1], 0, 1, 2, 3, 4, 5, 6, 7));
                    const v16f z = {};
                    acc[t] = mfma32(pa[ks], bv, ks ? acc[t] : z);
                    if (t < 2) { VRD(t + 2, 2 * ks); VRD(t + 2, 2 * ks + 1); }
                    if (t > 0 && ks >= 2 && ks < 6) { OSTORE(t - 1, 2 * (ks - 2)); OSTORE(t - 1, 2 * (ks - 2) + 1); }
                    if (t > 0 && ks == 5) { MEMBAR(); *(volatile LAS unsigned*)(L + fl + (t - 1) * 16) = tag; }
                    if (t == 0 && ks == 5) RAWBAR();
                    SCHED();
                }
            }
            STAMP(13); CYC1();
#pragma unroll
            for (int e = 0; e < 8; ++e) OSTORE(3, e);
            BAR();
            __builtin_amdgcn_s_setprio(0);
            if (UVA < 16) __builtin_amdgcn_s_sleep(3);
#pragma unroll
            for (int i = UVA; i < 16; ++i) vr[i >> 3][i & 7] = ld64_nt(rv, vo, (32 * (i >> 3) + (i & 7)) * 8192);
            SCHED();
            RAWBAR();
            v4u vf[2][4];
            frag4(vr[0], vf[0]); frag4(vr[1], vf[1]);
            int lr = QOFF + 13312 + (v >> 1) * 512 + (lane & 3) * 128 + kq * 16, cxo = lane < 16 ? WS_CTX + a * 512 + v * 128 + l15 * 8 : OOB, lo = QOFF + 12832 + (v >> 1) * 16;
            PIN(lr); PIN(cxo); PIN(lo);
            RAWBAR();
            {
                const v4u a0 = *(LAS v4u*)(L + lr), a1 = *(LAS v4u*)(L + lr + 64);
                const float rl = __builtin_amdgcn_rcpf(sum4(*(LAS v4f*)(L + lo)));
                v4f y[4] = {};
#pragma unroll
                for (int n = 0; n < 4; ++n) y[n] = mfma16(a1, vf[1][n], mfma16(a0, vf[0][n], y[n]));
                st64_sc1(v2u{pkbf(sum4(y[0]) * rl, sum4(y[1]) * rl), pkbf(sum4(y[2]) * rl, sum4(y[3]) * rl)}, rws, cxo);
            }
            STAMP(8);
        }
    }
    STAMP_FLUSH();
}

extern "C" void kernel_launch(void* const* d_in, const int* in_sizes, int n_in, void* d_out, int out_size, void* d_ws, size_t ws_size, hipStream_t stream) {
    fused_kernel<<<256, NT, 0, stream>>>((const u16*)d_in[0], (const u16*)d_in[1], (const u16*)d_in[2], (const u16*)d_in[3], (const u16*)d_in[4], (const u16*)d_in[5],
                                         (const u16*)d_in[7], (const u16*)d_in[8], (const u16*)d_in[9], (const u16*)d_in[10], (const u16*)d_in[11], (u16*)d_out, (unsigned char*)d_ws + WS_BASE);
}
static inline int stamp_group(int b) { return b >= 128 ? 0 : (b >= 64 ? 1 : (b >= 56 ? 4 : (b >= 18 ? 2 : 3))); }
```

```cpp
#include <hip/hip_runtime.h>
#include <hip/hip_bf16.h>

#define STAMPS 0
#define LATE_DELAY 470
#define DMAW 4
#define SLOW_EARLY 1
#define UVA 10
#define CH0 80
#define CHW 4
#define CH_DELAY 200
#define PFW 3
#define LATE_F 450
#define KVROT 23
#define TQ 475
#define UKA 384
#define G5AT 12
#define TQ2 475
#define FINW 6
#define QPF 0
#define CUE4 70
#define KPRE 16
#define LAG_OK 6
#define LAG_DUE 140
#define LATE_B2 700
#define CUE7 20
#define CUE8 30
#define WS_BASE 0
#define HINTI 0
typedef unsigned short u16;
typedef unsigned long long u64;
typedef unsigned v4u __attribute__((ext_vector_type(4)));
typedef unsigned v2u __attribute__((ext_vector_type(2)));
typedef float v4f __attribute__((ext_vector_type(4)));
typedef float v2f __attribute__((ext_vector_type(2)));
typedef float v16f __attribute__((ext_vector_type(16)));
typedef short v4s __attribute__((ext_vector_type(4)));
typedef unsigned short v2us __attribute__((ext_vector_type(2)));
typedef __bf16 v2bf __attribute__((ext_vector_type(2)));
typedef __bf16 v8bf __attribute__((ext_vector_type(8)));
#define LAS __attribute__((address_space(3)))
#define GAS __attribute__((address_space(1)))
typedef __amdgpu_buffer_rsrc_t rsrc_t;

constexpr int M = 576, DC = 512, SK = 16384, FF = 4608;
constexpr int NT = 512;
constexpr int QOFF = 131072;
constexpr int CODE_BYTES = 39424;
constexpr int SPIN_MAX = 1 << 16;
constexpr unsigned POISON = 0xFFFFFFFFu;
constexpr int OOB = 0x40000000;
constexpr int WS_EPOCH = 0;
constexpr int WS_CL = 4096;
constexpr int WS_H1 = WS_CL + 128 * 128;
constexpr int WS_ST = WS_H1 + 3 * 288 * 16;
constexpr int WS_QA = 256 * 1024;
constexpr int WS_PZ = WS_QA + 32768;
constexpr int WS_CTX = WS_PZ;
constexpr int WS_U5 = WS_CTX + 65536;
constexpr int WS_T5 = WS_U5 + 4608;
constexpr int WS_MRB = WS_T5 + 9216;
constexpr int WS_H6 = WS_MRB + 4608;
constexpr int WS_CK = 768 * 1024;
constexpr int WS_INB = 1 << 20;
constexpr int WS_STAMP = WS_INB + (4 << 20);
constexpr int WS_BYTES = WS_STAMP + 256 * 8 * 16 * 4;
static_assert(WS_ST + 32768 <= WS_QA && WS_H6 + 72 * 2304 <= WS_PZ + 8 * 32768 && WS_PZ + 8 * 32768 <= WS_CK && WS_CK + 131072 <= WS_INB, "ws map");
static_assert(WS_H1 % 128 == 0 && WS_ST % 128 == 0 && WS_U5 % 128 == 0 && WS_T5 % 128 == 0 && WS_MRB % 128 == 0 && WS_H6 % 128 == 0, "line aligned");

#define RSRC(p, bytes) __builtin_amdgcn_make_buffer_rsrc((void*)(p), 0, (bytes), 0x00020000)
#define AUX_NT 2
#define AUX_SC1 16
__device__ __forceinline__ v4u ld_nt(rsrc_t r, int off) { return __builtin_amdgcn_raw_buffer_load_b128(r, off, 0, AUX_NT); }
__device__ __forceinline__ v2u ld64_nt(rsrc_t r, int off, int so) { return __builtin_amdgcn_raw_buffer_load_b64(r, off, so, AUX_NT); }
__device__ __forceinline__ v4u ld_sc1(rsrc_t r, int off) { return __builtin_amdgcn_raw_buffer_load_b128(r, off, 0, AUX_SC1); }
__device__ __forceinline__ unsigned ld32_sc1(rsrc_t r, int off) { return __builtin_amdgcn_raw_buffer_load_b32(r, off, 0, AUX_SC1); }
__device__ __forceinline__ void st_sc1(v4u v, rsrc_t r, int off) { __builtin_amdgcn_raw_buffer_store_b128(v, r, off, 0, AUX_SC1); }
__device__ __forceinline__ void st64_sc1(v2u v, rsrc_t r, int off) { __builtin_amdgcn_raw_buffer_store_b64(v, r, off, 0, AUX_SC1); }
__device__ __forceinline__ void st32_sc1(unsigned v, rsrc_t r, int off) { __builtin_amdgcn_raw_buffer_store_b32(v, r, off, 0, AUX_SC1); }
template <int NTP = 1> __device__ __forceinline__ void dma16(rsrc_t r, int lds, int voff, int soff) {
    if (NTP == 2) asm volatile("s_mov_b32 m0, %0\n\ts_nop 0\n\tbuffer_load_dwordx4 %1, %2, %3 offen sc1 lds" :: "s"(lds), "v"(voff), "s"(r), "s"(soff) : "memory");
    else if (NTP == 1) asm volatile("s_mov_b32 m0, %0\n\ts_nop 0\n\tbuffer_load_dwordx4 %1, %2, %3 offen nt lds" :: "s"(lds), "v"(voff), "s"(r), "s"(soff) : "memory");
    else if (NTP == 0) asm volatile("s_mov_b32 m0, %0\n\ts_nop 0\n\tbuffer_load_dwordx4 %1, %2, %3 offen lds" :: "s"(lds), "v"(voff), "s"(r), "s"(soff) : "memory");
}
#define MEMBAR() asm volatile("" ::: "memory")
#define DRAIN() asm volatile("s_waitcnt vmcnt(0)" ::: "memory")
#define SCHED() __builtin_amdgcn_sched_barrier(0)
#define PIN(x) asm volatile("" : "+v"(x))
#define LGKM0() do { MEMBAR(); __builtin_amdgcn_s_waitcnt(0xC07F); MEMBAR(); } while (0)
#define BAR() do { LGKM0(); asm volatile("s_barrier" ::: "memory"); } while (0)
#define PAGE() asm volatile("s_branch 1f\n\t.p2align 12\n1:" ::: "memory")
#define VWAIT(n) do { unsigned kd_, ib_; asm volatile("1:\n\tv_mov_b32 %0, %0\n\ts_getreg_b32 %1, hwreg(HW_REG_IB_STS, 0, 4)\n\tv_mov_b32 %0, %0\n\ts_cmp_gt_u32 %1, %2\n\tv_mov_b32 %0, %0\n\ts_cbranch_scc1 1b" \
        : "=&v"(kd_), "=&s"(ib_) : "i"(n) : "scc", "memory"); } while (0)
#define PIN8(p, o) asm volatile("" : "+v"(p[o]), "+v"(p[o + 1]), "+v"(p[o + 2]), "+v"(p[o + 3]), "+v"(p[o + 4]), "+v"(p[o + 5]), "+v"(p[o + 6]), "+v"(p[o + 7]))
#define RAWBAR() do { asm volatile("s_barrier" ::: "memory"); } while (0)

__device__ __forceinline__ float u2f(unsigned u) { return __builtin_bit_cast(float, u); }
__device__ __forceinline__ unsigned f2u(float f) { return __builtin_bit_cast(unsigned, f); }
__device__ __forceinline__ float bflo(unsigned u) { return u2f(u << 16); }
__device__ __forceinline__ float bfhi(unsigned u) { return u2f(u & 0xFFFF0000u); }
__device__ __forceinline__ unsigned pkbf(float lo, float hi) { v2f p = {lo, hi}; v2bf q = __builtin_convertvector(p, v2bf); return __builtin_bit_cast(unsigned, q); }
__device__ __forceinline__ float rb(float f) { return bflo(pkbf(f, f)); }
__device__ __forceinline__ v4f rb4(v4f f) { const unsigned a = pkbf(f.x, f.y), b = pkbf(f.z, f.w); return v4f{bflo(a), bfhi(a), bflo(b), bfhi(b)}; }
__device__ __forceinline__ float dot2(unsigned a, unsigned b, float c) { return __builtin_amdgcn_fdot2_f32_bf16(__builtin_bit_cast(v2bf, a), __builtin_bit_cast(v2bf, b), c, false); }
__device__ __forceinline__ unsigned perm(unsigned hi, unsigned lo, unsigned sel) { return __builtin_amdgcn_perm(hi, lo, sel); }
__device__ __forceinline__ unsigned umax(unsigned a, unsigned b) { return a > b ? a : b; }
__device__ __forceinline__ v4f mfma16(v4u a, v4u b, v4f c) { return __builtin_amdgcn_mfma_f32_16x16x32_bf16(__builtin_bit_cast(v8bf, a), __builtin_bit_cast(v8bf, b), c, 0, 0, 0); }
__device__ __forceinline__ v16f mfma32(v4u a, v4u b, v16f c) { return __builtin_amdgcn_mfma_f32_32x32x16_bf16(__builtin_bit_cast(v8bf, a), __builtin_bit_cast(v8bf, b), c, 0, 0, 0); }
__device__ __forceinline__ float sum4(v4f a) { float s = a.x + a.y, t = a.z; asm("" : "+v"(s)); t += a.w; asm("" : "+v"(t)); s += t; asm("" : "+v"(s)); return s; }
__device__ __forceinline__ v4u pack4(float a, float b, float c, float d, unsigned tag) {
    const unsigned ud = f2u(d);
    v4u g; g.x = perm(f2u(a), ud, 0x07060501u); g.y = perm(f2u(b), ud, 0x07060502u); g.z = perm(f2u(c), ud, 0x07060503u); g.w = tag; return g;
}
__device__ __forceinline__ v4f unpack4(v4u g) {
    const unsigned gx = g.x, gy = g.y, gz = g.z;
    const unsigned t = perm(gy, gx, 0x0c04000cu);
    const unsigned d = perm(gz, t, 0x04020100u);
    v4f r = {u2f(gx & 0xFFFFFF00u), u2f(gy & 0xFFFFFF00u), u2f(gz & 0xFFFFFF00u), u2f(d)}; return r;
}
__device__ __forceinline__ v4f unpack4n(v4u g) {
    const unsigned gx = g.x, gy = g.y, gz = g.z;
    v4f r = {u2f(gx), u2f(gy), u2f(gz), u2f(perm(gz, perm(gy, gx, 0x0c04000cu), 0x04020100u))}; return r;
}
__device__ __forceinline__ float red32(float a, float b) { auto r = __builtin_amdgcn_permlane32_swap(f2u(a), f2u(b), false, false); const unsigned x = r[0], y = r[1]; return u2f(x) + u2f(y); }
__device__ __forceinline__ float red16(float a, float b) { auto r = __builtin_amdgcn_permlane16_swap(f2u(a), f2u(b), false, false); const unsigned x = r[0], y = r[1]; return u2f(x) + u2f(y); }
__device__ __forceinline__ float max32(float a, float b) { auto r = __builtin_amdgcn_permlane32_swap(f2u(a), f2u(b), false, false); const unsigned x = r[0], y = r[1]; return fmaxf(u2f(x), u2f(y)); }
__device__ __forceinline__ float max16(float a) { auto r = __builtin_amdgcn_permlane16_swap(f2u(a), f2u(a), false, false); const unsigned x = r[0], y = r[1]; return fmaxf(u2f(x), u2f(y)); }
template <int CTRL> __device__ __forceinline__ float dpp(float v) { return u2f((unsigned)__builtin_amdgcn_update_dpp(0, (int)f2u(v), CTRL, 0xf, 0xf, true)); }
__device__ __forceinline__ float add_x8(float v) { return v + dpp<0x128>(v); }
__device__ __forceinline__ float add_r4(float v) { return v + dpp<0x124>(v); }
__device__ __forceinline__ float add_x2(float v) { return v + dpp<0x4E>(v); }
__device__ __forceinline__ float add_x1(float v) { return v + dpp<0xB1>(v); }
__device__ __forceinline__ float row_sum(float v) { return add_r4(add_x8(add_x2(add_x1(v)))); }
__device__ __forceinline__ float row_max(float v) { v = fmaxf(v, dpp<0xB1>(v)); v = fmaxf(v, dpp<0x4E>(v)); v = fmaxf(v, dpp<0x128>(v)); return fmaxf(v, dpp<0x124>(v)); }
template <int CTRL> __device__ __forceinline__ unsigned addu_dpp(unsigned v) { return v + (unsigned)__builtin_amdgcn_update_dpp(0, (int)v, CTRL, 0xf, 0xf, true); }
__device__ __forceinline__ unsigned redu16(unsigned a, unsigned b) { auto r = __builtin_amdgcn_permlane16_swap(a, b, false, false); const unsigned x = r[0], y = r[1]; return x + y; }
__device__ __forceinline__ unsigned redu32(unsigned a, unsigned b) { auto r = __builtin_amdgcn_permlane32_swap(a, b, false, false); const unsigned x = r[0], y = r[1]; return x + y; }
__device__ __forceinline__ float wave_sum(float v) { v = row_sum(v); v = red16(v, v); return red32(v, v); }
__device__ __forceinline__ void interleave(v4u& a, v4u& b) {
    const unsigned a0 = a.x, a1 = a.y, a2 = a.z, a3 = a.w, b0 = b.x, b1 = b.y, b2 = b.z, b3 = b.w;
    a.x = perm(b0, a0, 0x05040100u); a.y = perm(b0, a0, 0x07060302u); a.z = perm(b1, a1, 0x05040100u); a.w = perm(b1, a1, 0x07060302u);
    b.x = perm(b2, a2, 0x05040100u); b.y = perm(b2, a2, 0x07060302u); b.z = perm(b3, a3, 0x05040100u); b.w = perm(b3, a3, 0x07060302u);
}
__device__ __forceinline__ void frag4(const v2u* r, v4u* f) {
    unsigned o[4][4];
#pragma unroll
    for (int p = 0; p < 4; ++p) {
        const unsigned ax = r[2 * p].x, ay = r[2 * p].y, bx = r[2 * p + 1].x, by = r[2 * p + 1].y;
        o[0][p] = perm(bx, ax, 0x05040100u); o[1][p] = perm(bx, ax, 0x07060302u); o[2][p] = perm(by, ay, 0x05040100u); o[3][p] = perm(by, ay, 0x07060302u);
    }
#pragma unroll
    for (int n = 0; n < 4; ++n) { f[n] = v4u{o[n][0], o[n][1], o[n][2], o[n][3]}; PIN(f[n]); }
}
__device__ __forceinline__ void dot8(float (&acc)[8], const v4u& a, const v4u& b, unsigned xp) {
    const unsigned a0 = a.x, a1 = a.y, a2 = a.z, a3 = a.w, b0 = b.x, b1 = b.y, b2 = b.z, b3 = b.w;
    acc[0] = dot2(a0, xp, acc[0]); acc[1] = dot2(a1, xp, acc[1]); acc[2] = dot2(a2, xp, acc[2]); acc[3] = dot2(a3, xp, acc[3]);
    acc[4] = dot2(b0, xp, acc[4]); acc[5] = dot2(b1, xp, acc[5]); acc[6] = dot2(b2, xp, acc[6]); acc[7] = dot2(b3, xp, acc[7]);
}
__device__ __forceinline__ void dot8z(float (&acc)[8], const v4u& a, unsigned slo, unsigned shi) {
    const unsigned a0 = a.x, a1 = a.y, a2 = a.z, a3 = a.w;
    acc[0] = dot2(a0, slo, acc[0]); acc[1] = dot2(a0, shi, acc[1]); acc[2] = dot2(a1, slo, acc[2]); acc[3] = dot2(a1, shi, acc[3]);
    acc[4] = dot2(a2, slo, acc[4]); acc[5] = dot2(a2, shi, acc[5]); acc[6] = dot2(a3, slo, acc[6]); acc[7] = dot2(a3, shi, acc[7]);
}
#define ZERO8(acc) float acc[8] = {0.f, 0.f, 0.f, 0.f, 0.f, 0.f, 0.f, 0.f}; asm volatile("" : "+v"(acc[0]), "+v"(acc[1]), "+v"(acc[2]), "+v"(acc[3]), "+v"(acc[4]), "+v"(acc[5]), "+v"(acc[6]), "+v"(acc[7]))
__device__ __forceinline__ float red8cols(const float (&a)[8], int lane) {
    const float b0 = red32(a[0], a[4]), b1 = red32(a[1], a[5]), b2 = red32(a[2], a[6]), b3 = red32(a[3], a[7]);
    const float c0 = red16(b0, b2), c1 = red16(b1, b3);
    const float d0 = add_x8(c0), d1 = add_x8(c1);
    return (lane & 8) ? d1 : d0;
}
__device__ __forceinline__ u64 rt();
template <bool TAGGED> __device__ __forceinline__ void cue(rsrc_t rws, int off, unsigned tag, int ticks) {
#pragma unroll 1
    for (int sp = 0; sp < SPIN_MAX; ++sp) {
        MEMBAR();
        const unsigned f = __builtin_amdgcn_readfirstlane(ld32_sc1(rws, off));
        if (TAGGED ? f == tag : f != POISON) { if (sp == 0) ticks = 0; break; }
        __builtin_amdgcn_s_sleep(3);
    }
    const u64 t0 = rt();
    while (rt() - t0 < (u64)ticks) __builtin_amdgcn_s_sleep(1);
}
__device__ __forceinline__ float silu_bf(float v) { return rb(v * __builtin_amdgcn_rcpf(1.f + __expf(-v))); }
__device__ __forceinline__ u64 rt() { return __builtin_amdgcn_s_memrealtime(); }
#define STAMP(i)
#define STAMP_FLUSH()

__global__ void __attribute__((aligned(4096))) __launch_bounds__(NT, 2) fused_kernel(const u16* a_x, const u16* a_ckv, const u16* a_wq, const u16* a_wuk, const u16* a_wuv, const u16* a_wo, const u16* a_wg, const u16* a_wu,
                                                      const u16* a_wd, const u16* a_c, const u16* a_wmod, u16* a_out, unsigned char* a_ws) {
    __shared__ __attribute__((aligned(16))) unsigned char smem[163840];
    LAS unsigned char* L = (LAS unsigned char*)smem;
    typedef unsigned v16u __attribute__((ext_vector_type(16)));
    typedef unsigned v8u __attribute__((ext_vector_type(8)));
    v16u ka0; v8u ka1; v2u ka2; u64 pc0;
    asm volatile("s_load_dwordx16 %0, %4, 0x0\n\ts_load_dwordx8 %1, %4, 0x40\n\ts_load_dwordx2 %2, %4, 0x60\n\ts_getpc_b64 %3"
                 : "=&s"(ka0), "=&s"(ka1), "=&s"(ka2), "=&s"(pc0) : "s"(__builtin_amdgcn_kernarg_segment_ptr()));
#define KWAIT() do { SCHED(); asm volatile("s_waitcnt lgkmcnt(0)" : "+s"(ka0), "+s"(ka1), "+s"(ka2)); } while (0)
#define KPTR(v, i) ((u64)v[2 * (i)] | ((u64)v[2 * (i) + 1] << 32))
    const u64 t_start = rt();
    const int bid = blockIdx.x, tid = threadIdx.x, lane = tid & 63, w = __builtin_amdgcn_readfirstlane(tid >> 6);
    const int l15 = lane & 15, kq = lane >> 4, half = lane >> 5;
    const int a = bid & 127, xcd = bid & 7, jj = (bid >> 3) & 7;
    const int hh = 4 * xcd + (jj >> 1), cj = jj & 1;
    const int job0 = bid >= 128 ? 4 : (bid < 64 ? 2 : 0) + (w >> 2);
    int job = job0;
    unsigned tag0, xb, sc[6]; v4u s1w[6], xa; v2u xm;
    const int s1t = bid - 64, kp = (s1t * 57) >> 10, nt1 = s1t - 18 * kp;
    constexpr int HROW = 147456, QROW = HROW + 2304, SCR = 155648, ASCR = 120 * 1024;
    asm volatile("" : "+s"(job));
    if (job == 0) {
        const int r1 = 192 * kp + 48 * w + (lane >> 3);
        int s0 = nt1 * 128 + r1 * 6912 + (lane & 7) * 16, s1 = r1 * 2;
        PIN(s0); PIN(s1);
        KWAIT();
        const rsrc_t rmod = RSRC(KPTR(ka1, 2), M * 6 * M * 2), rc = RSRC(KPTR(ka1, 1), M * 2);
#pragma unroll
        for (int i = 0; i < 6; ++i) asm volatile("buffer_load_ushort %0, %1, %2, 0 offen offset:%3" : "=v"(sc[i]) : "v"(s1), "s"(rc), "i"(16 * i) : "memory");
        asm volatile("global_load_dword %0, %1, %2 sc1" : "=v"(tag0) : "v"(WS_EPOCH), "s"(KPTR(ka2, 0)) : "memory");
#pragma unroll
        for (int i = 0; i < 6; ++i) asm volatile("buffer_load_dwordx4 %0, %1, %2, %3 offen nt" : "=v"(s1w[i]) : "v"(s0), "s"(rmod), "s"(8 * i * 6912) : "memory");
        if (s1t >= 54 && s1t < 63) {
            const v4u poison = {POISON, POISON, POISON, POISON};
#pragma unroll 1
            for (int i = 0; i < 8; ++i) __builtin_amdgcn_raw_buffer_store_b128(poison, RSRC(KPTR(ka2, 0), WS_BYTES), lane * 16, WS_QA + ((s1t - 54) * 4 + w) * 8192 + i * 1024, AUX_SC1);
        }
        SCHED();
        STAMP(10);
    }
    int po = xcd * 1024 + lane * 16;
    const int pn = 16 * w + (a >> 3);
    asm volatile("" : "+s"(job));
    if (job == 4) {
        PIN(po);
        if (tid < 24 && (a >> 3) * 3072 + tid * 128 < CODE_BYTES)
            asm volatile("s_mov_b32 m0, %2\n\ts_nop 0\n\tglobal_load_lds_dword %0, %1" :: "v"((a >> 3) * 3072 + tid * 128), "s"(pc0 & ~4095ull), "i"(ASCR) : "memory");
        const int at = xcd + 8 * (a >> 4), ak = (at * 57) >> 10;
        int ao = (at - 18 * ak) * 128 + (192 * ak + 48 * (w & 3) + 16 + 2 * (a & 8) + 2 * (w & 4) + (lane >> 3)) * 6912 + (lane & 7) * 16;
        PIN(ao);
        KWAIT();
        dma16<0>(RSRC(KPTR(ka1, 2), M * 6 * M * 2), ASCR + w * 1024, ao, 0);
#pragma unroll
        for (int i = 0; i < 3; ++i) dma16<0>(RSRC(KPTR(ka0, 2), M * 8192), ASCR + w * 1024, po, (96 + (pn >> 1) + 288 * (pn & 1) + 64 * i) * 8192);
    }
    asm volatile("" : "+s"(job));
    if (job == 2) {
        int x0 = lane * 16, x1 = lane * 4, x2 = (36 * w + (lane < 36 ? lane : 35)) * 8;
        PIN(x0); PIN(x1); PIN(x2);
        KWAIT();
        const rsrc_t rx = RSRC(KPTR(ka0, 0), M * 2);
        asm volatile("buffer_load_dwordx4 %0, %4, %7, 0 offen\n\tbuffer_load_dword %1, %5, %7, 0 offen offset:1024\n\tbuffer_load_dwordx2 %2, %6, %7, 0 offen\n\tglobal_load_dword %3, %8, %9 sc1\n\ts_barrier"
                     : "=&v"(xa), "=&v"(xb), "=&v"(xm), "=&v"(tag0) : "v"(x0), "v"(x1), "v"(x2), "s"(rx), "v"(WS_EPOCH), "s"(KPTR(ka2, 0)) : "memory");
    }
    int qv;
#define WQ_LOADS(lo, hi) do { const rsrc_t rq = RSRC(KPTR(ka0, 2), M * 8192); \
        _Pragma("unroll 1") for (int i_ = (lo); i_ < (hi); ++i_) dma16<0>(rq, (4 * i_ + w - 4) * 1024, qv, (4 * (i_ + ((lo) < 18 ? 18 : -18) * cj) + w - 4) * 32768); } while (0)
    asm volatile("" : "+s"(job));
    if (job & 1) {
        qv = kq * 8192 + hh * 256 + ((l15 ^ (2 * kq) ^ ((w & 2) << 2)) * 16);
        PIN(qv);
        KWAIT();
        RAWBAR();
        if (job == 3) {
            WQ_LOADS(0, 16);
        }
    }
    int kvo, kvs, kvl;
#define KV_LOADS(n, W) do { const rsrc_t rkv = RSRC(KPTR(ka0, 1), SK * DC * 2); \
        _Pragma("unroll 1") for (int i_ = 0; i_ < (n); ++i_) { \
            dma16<1>(rkv, kvl, kvo, kvs); \
            kvo ^= 32; kvs = ((kvs + 1048576) & ~0xFE000) | ((kvs + KVROT * 8192) & 0xFE000); kvl += 8192; \
            asm volatile("s_waitcnt vmcnt(%0)" :: "i"((W) - 1) : "memory"); } } while (0)
#define UK_ROW(n) (16 * ((n) & 28) + 4 * ((n) >> 5) + ((n) & 3))
    asm volatile("" : "+s"(job));
    if (job == 0) {
        const rsrc_t rws = RSRC(KPTR(ka2, 0), WS_BYTES);
        if (s1t >= 54 && s1t < 63) {
            asm volatile("s_waitcnt vmcnt(0)" : "+v"(tag0) :: "memory");
            __builtin_amdgcn_raw_buffer_store_b32(tag0 + 1u, rws, lane * 4, WS_CL + ((s1t - 54) * 4 + w) * 256, AUX_SC1);
        }
        ZERO8(acc);
        int ra = (w * 64 + (lane & 7) * 8 + (lane >> 3)) * 4, rr = l15 * 16, so = (s1t < 54 && lane < 16) ? WS_H1 + kp * 4608 + (nt1 * 16 + lane) * 16 : OOB;
        PIN(ra); PIN(rr); PIN(so);
        SCHED();
        unsigned slo[6], shi[6];
        asm volatile("s_waitcnt vmcnt(6)" : "+v"(sc[0]), "+v"(sc[1]), "+v"(sc[2]), "+v"(sc[3]), "+v"(sc[4]), "+v"(sc[5]), "+v"(tag0));
        tag0 += 1u; PIN(tag0);
#pragma unroll
        for (int i = 0; i < 6; ++i) { shi[i] = f2u(silu_bf(bflo(sc[i]))); slo[i] = shi[i] >> 16; PIN(shi[i]); PIN(slo[i]); }
        SCHED();
#pragma unroll
        for (int i = 0; i < 6; ++i) { asm volatile("s_waitcnt vmcnt(%1)" : "+v"(s1w[i]) : "i"(5 - i)); dot8z(acc, s1w[i], slo[i], shi[i]); SCHED(); }
        *(LAS float*)(L + ra) = red8cols(acc, lane);
        BAR();
        if (w == 0) {
            LAS v4f* R = (LAS v4f*)(L + rr);
            const v4f s = (R[0] + R[16]) + (R[32] + R[48]);
            st_sc1(pack4(s.x, s.y, s.z, s.w, tag0), rws, so);
        }
    }
    const u64 x = KPTR(ka0, 0), ckv = KPTR(ka0, 1), wq = KPTR(ka0, 2), wuv = KPTR(ka0, 4), wo = KPTR(ka0, 5), wg = KPTR(ka0, 6), wu = KPTR(ka0, 7);
    const u64 wd = KPTR(ka1, 0), cc = KPTR(ka1, 1), wmod = KPTR(ka1, 2);
    GAS u16* out = (GAS u16*)KPTR(ka1, 3);
    GAS unsigned char* ws = (GAS unsigned char*)KPTR(ka2, 0);
    const rsrc_t rws = RSRC((u64)ws, WS_BYTES), rx = RSRC(x, M * 2), rmod = RSRC(wmod, M * 6 * M * 2), rc = RSRC(cc, M * 2);
    SCHED();
    STAMP(9);
    asm volatile("" : "+s"(job));

    if (job < 4) {
        const int b = bid;
        LAS float* red = (LAS float*)L;
        const int clo = WS_CL + (lane < 36 ? lane : 0) * 256;
        unsigned tag;
        unsigned lmask = SLOW_EARLY ? (0x101u << ((3u - (unsigned)(wg >> 7) - xcd) & 7u)) | (0x101u << ((3u - (unsigned)(wu >> 7) - xcd) & 7u)) : 0u;
        const int lq = __builtin_ctz(~lmask);
        const unsigned smask = ((lmask | (lq ? 1u << lq : 0u)) >> 1) & 255u;
        const bool slowf = b >= 64 && ((smask >> jj) & 1u);
        if (job < 2) {
            RAWBAR();
#pragma unroll 1
            for (int n = UKA + 8 * w + jj; n < 512; n += 64) dma16<0>(RSRC(KPTR(ka0, 3), DC * 8192), SCR + w * 1024, po, UK_ROW(n) * 8192);
            if (CH_DELAY) { while (rt() - t_start < (u64)CH_DELAY) __builtin_amdgcn_s_sleep(1); }
            const rsrc_t rkv = RSRC(ckv, SK * DC * 2);
            const int hn = 8 - __builtin_popcount(smask), hr = __builtin_popcount(~smask & ((1u << jj) - 1u));
#pragma unroll 1
            for (int i = slowf ? 256 : 2 * CH0 + hr; i < 256; i += hn) {
                dma16<0>(rkv, SCR + w * 1024, lane * 16, (i >> 4) * 1048576 + (((xcd + 8 * (i & 15) + KVROT * (i >> 4)) & 127) * 8 + w) * 1024);
                asm volatile("s_waitcnt vmcnt(%0)" :: "i"(CHW - 1) : "memory");
            }
            tag = ld32_sc1(rws, WS_EPOCH) + 1u;
            if (w == 0) {
#pragma unroll 1
                for (int sp = 0; sp < SPIN_MAX; ++sp) { MEMBAR(); if (__all(ld32_sc1(rws, clo) == tag)) break; }
            }
            DRAIN(); RAWBAR();
            STAMP(5);
        } else if (job == 2) {
            PAGE();
            const int j36 = lane < 36 ? lane : 35;
            float n0, n1, n2, n3;
            {
                asm volatile("s_waitcnt vmcnt(0)" : "+v"(xa), "+v"(xb), "+v"(xm), "+v"(tag0));
                tag = tag0;
                const unsigned a0 = xa.x, a1 = xa.y, a2 = xa.z, a3 = xa.w, m0 = xm.x, m1 = xm.y;
                float vv[10] = {bflo(a0), bfhi(a0), bflo(a1), bfhi(a1), bflo(a2), bfhi(a2), bflo(a3), bfhi(a3), bflo(xb), bfhi(xb)};
                float s = 0.f;
#pragma unroll
                for (int i = 0; i < 10; ++i) s += vv[i];
                const float mean = wave_sum(s) * (1.f / M);
                float q = 0.f;
#pragma unroll
                for (int i = 0; i < 8; ++i) q += (vv[i] - mean) * (vv[i] - mean);
                if (lane < 32) q += (vv[8] - mean) * (vv[8] - mean) + (vv[9] - mean) * (vv[9] - mean);
                const float var = rb(wave_sum(q) * (1.f / M));
                const float den = rb(__builtin_amdgcn_sqrtf(rb(var + 1e-5f))), mb = rb(mean);
                n0 = rb(rb(bflo(m0) - mb) / den); n1 = rb(rb(bfhi(m0) - mb) / den); n2 = rb(rb(bflo(m1) - mb) / den); n3 = rb(rb(bfhi(m1) - mb) / den);
                PIN(n0); PIN(n1); PIN(n2); PIN(n3);
                tag += 1u;
                *(LAS unsigned*)(L + SCR + 4096) = tag;
            }
            v4u bf[2][18];
            v4f a0 = {0.f, 0.f, 0.f, 0.f}, a1 = {0.f, 0.f, 0.f, 0.f};
            int qa = QROW + (32 * w + l15) * 2, aa = HROW + 576 * cj + kq * 16;
            const int q = l15 >> 2, p = lane & 3;
            int t0 = (8 * kq + q) * 256 + (((4 * w + (p >> 1)) ^ (2 * q) ^ ((kq & 1) << 3)) * 16) + 8 * (p & 1), t1 = t0 ^ 32;
            int o = WS_H1 + (36 * w + j36) * 16, hw = HROW + (36 * w + j36) * 8, fo = w == 0 ? clo : WS_CL;
            PIN(t0); PIN(t1); PIN(qa); PIN(aa); PIN(o); PIN(hw); PIN(fo);
#define WQ_FRAGS(lo, hi) do { _Pragma("unroll") for (int k = (lo); k < (hi); ++k) _Pragma("unroll") for (int m = 0; m < 2; ++m) { \
                    const int tb = (m ? t1 : t0) + (k / 6) * 49152; \
                    const v4s lo_ = __builtin_amdgcn_ds_read_tr16_b64_v4i16((LAS v4s*)(L + tb + (k % 6) * 8192)); \
                    const v4s hi_ = __builtin_amdgcn_ds_read_tr16_b64_v4i16((LAS v4s*)(L + tb + (k % 6) * 8192 + 1024)); \
                    bf[m][k] = __builtin_bit_cast(v4u, __builtin_shufflevector(lo_, hi_, 0, 1, 2, 3, 4, 5, 6, 7)); } SCHED(); } while (0)
#define WQ_MFMA(lo, hi) do { v4u av[18]; \
                    _Pragma("unroll") for (int k = (lo); k < (hi) && k < (lo) + 6; ++k) av[k] = *(LAS v4u*)(L + aa + k * 64); \
                    SCHED(); \
                    _Pragma("unroll") for (int k = (lo); k < (hi); ++k) { \
                        a0 = mfma16(av[k], bf[0][k], a0); a1 = mfma16(av[k], bf[1][k], a1); \
                        if (k + 6 < (hi)) av[k + 6] = *(LAS v4u*)(L + aa + (k + 6) * 64); \
                        SCHED(); } } while (0)
            STAMP(10);
            BAR();
            WQ_FRAGS(0, 5);
            RAWBAR();
            v4u g[6]; unsigned cf;
#define H1_LOADS() do { _Pragma("unroll") for (int i = 0; i < 6; ++i) g[i] = __builtin_amdgcn_raw_buffer_load_b128(rws, o, 2304 * i, AUX_SC1); } while (0)
#define H1_OK() __all(g[0].w == tag && g[1].w == tag && g[2].w == tag && g[3].w == tag && g[4].w == tag && g[5].w == tag)
            WQ_FRAGS(5, 10);
            BAR();
            WQ_FRAGS(10, 15);
            H1_LOADS(); cf = ld32_sc1(rws, fo); SCHED();
            RAWBAR();
            STAMP(12);
            WQ_FRAGS(15, 18);
            if (__builtin_expect(!H1_OK(), 0)) {
#pragma unroll 1
                for (int sp = 0; sp < SPIN_MAX; ++sp) { MEMBAR(); H1_LOADS(); if (H1_OK()) break; }
            }
            STAMP(11);
            {
                const v4f sa = unpack4n(g[0]) + unpack4n(g[2]) + unpack4n(g[4]), ca = unpack4n(g[1]) + unpack4n(g[3]) + unpack4n(g[5]);
                const unsigned s01 = pkbf(sa.x, sa.y), s23 = pkbf(sa.z, sa.w), c01 = pkbf(ca.x, ca.y), c23 = pkbf(ca.z, ca.w);
                const unsigned d01 = pkbf(1.f + bflo(c01), 1.f + bfhi(c01)), d23 = pkbf(1.f + bflo(c23), 1.f + bfhi(c23));
                const unsigned t01 = pkbf(n0 * bflo(d01), n1 * bfhi(d01)), t23 = pkbf(n2 * bflo(d23), n3 * bfhi(d23));
                const v2u hv = {pkbf(bflo(t01) + bflo(s01), bfhi(t01) + bfhi(s01)), pkbf(bflo(t23) + bflo(s23), bfhi(t23) + bfhi(s23))};
                *(LAS v2u*)(L + hw) = hv; *(LAS v2u*)(L + hw + 1152) = hv;
            }
            BAR();
            STAMP(2);
            WQ_MFMA(0, 18);
            const unsigned qp = pkbf(a0.x, a1.x);
            *(LAS u16*)(L + qa) = (u16)qp; *(LAS u16*)(L + qa + 32) = (u16)(qp >> 16);
            if (__builtin_expect(!__all(cf == tag), 0)) {
#pragma unroll 1
                for (int sp = 0; sp < SPIN_MAX; ++sp) { MEMBAR(); if (__all(ld32_sc1(rws, fo) == tag)) break; }
            }
            BAR();
            STAMP(3);
            RAWBAR();
        } else {
            PAGE();
            const int v = w - 4;
            int qr = QROW + kq * 16;
            int qs = l15 < 4 ? WS_QA + hh * 1024 + (256 * cj + 64 * v + 16 * l15 + 4 * kq) * 2 : OOB;
            int uko = kq * 8192 + ((l15 ^ kq) * 16), ur = v * 16384 + l15 * 256 + ((kq ^ l15) * 16);
            const int uks = (256 * cj + 64 * v) * 8192 + hh * 256;
            const rsrc_t rk = RSRC(KPTR(ka0, 3), DC * 8192);
#define UK_LOADS(lo, hi) _Pragma("unroll 1") for (int i_ = (lo); i_ < (hi); ++i_) dma16<0>(rk, v * 16384 + i_ * 1024, uko ^ ((i_ & 3) << 6), uks + i_ * 32768)
            PIN(qr); PIN(qs); PIN(uko); PIN(ur);
            asm volatile("s_waitcnt vmcnt(6)\n\ts_barrier" ::: "memory");
            STAMP(10);
            tag = *(LAS unsigned*)(L + SCR + 4096);
            WQ_LOADS(16, 18); WQ_LOADS(18, 26);
            asm volatile("s_waitcnt vmcnt(6)\n\ts_barrier" ::: "memory");
            WQ_LOADS(26, 36);
            asm volatile("s_waitcnt vmcnt(6)\n\ts_barrier" ::: "memory");
            UK_LOADS(0, 6);
            asm volatile("s_waitcnt vmcnt(6)\n\ts_barrier" ::: "memory");
            UK_LOADS(6, G5AT);
            RAWBAR();
            UK_LOADS(G5AT, 16);
            DRAIN();
            STAMP(11);
            v4u uf[4][4];
#pragma unroll
            for (int t = 0; t < 4; ++t)
#pragma unroll
                for (int k = 0; k < 4; ++k) uf[t][k] = *(LAS v4u*)(L + (ur ^ (k << 6)) + t * 4096);
            SCHED();
            RAWBAR();
            STAMP(4);
            {
                v4u qf[4];
#pragma unroll
                for (int k = 0; k < 4; ++k) qf[k] = *(LAS v4u*)(L + qr + k * 64);
                v4f y[4] = {};
#pragma unroll
                for (int t = 0; t < 4; ++t)
#pragma unroll
                    for (int k = 0; k < 4; ++k) y[t] = mfma16(uf[t][k], qf[k], y[t]);
                const v4f z = ((lane & 2) ? ((lane & 1) ? y[3] : y[2]) : ((lane & 1) ? y[1] : y[0])) * (0.08838834764831845f * 1.4426950408889634f);
                st64_sc1(v2u{pkbf(z.x, z.y), pkbf(z.z, z.w)}, rws, qs);
            }
            STAMP(5);
            RAWBAR();
        }
        if (!slowf) { const u64 dl = b >= 56 ? LATE_F : LATE_DELAY; while (rt() - t_start < dl) __builtin_amdgcn_s_sleep(1); }
        if (QPF && b >= 24 && b < 56) {
            const rsrc_t rd = RSRC(wd, M * FF * 2);
#pragma unroll 1
            for (int k = 576 - QPF + w * 4 + jj - 3; k < 576; k += 32) {
                const int fj = (k * 7282) >> 16, rr = k - 9 * fj;
                dma16<0>(rd, SCR + w * 1024, lane * 16, (64 * (9 * xcd + rr) + (fj & 32) + 8 * (fj & 3) + ((fj >> 2) & 7)) * 1152);
            }
        }
        if (b >= 56) {
            const int f = 9 * xcd + (b < 64 ? lq : (jj + 1 == lq ? 0 : jj + 1));
            constexpr int FA = 147456, FROW = 1216, FS = FA + 3 * FROW, FF1 = FS + 32, FG = FS + 64, FD = 155648;
            if (w < 4) {
                {
                    const rsrc_t rg = RSRC((w & 2) ? wu : wg, M * FF * 2);
                    const int r8 = lane >> 3, base = (288 * (w & 1) + r8) * 9216 + f * 128;
                    const int v0 = base + (((lane & 7) ^ (r8 & 2)) * 16), v1 = base + (((lane & 7) ^ ((r8 & 2) | 4)) * 16);
#pragma unroll
                    for (int i = 0; i < 36; ++i) {
                        dma16<1>(rg, w * 36864 + i * 1024, (i & 1) ? v1 : v0, 8 * i * 9216);
                        if (i == 30) RAWBAR();
                    }
                    const rsrc_t rd = RSRC(wd, M * FF * 2);
                    const int db = (64 * f + 16 * w + r8) * 1152 + 1024;
                    dma16(rd, FD + w * 2048, db + (((lane & 7) ^ (r8 & 2)) * 16), 0);
                    dma16(rd, FD + w * 2048 + 1024, db + (((lane & 7) ^ ((r8 & 2) | 4)) * 16), 8 * 1152);
                    DRAIN();
                }
                RAWBAR();
                STAMP(6);
                v4u bf[2][18], ef[2];
                {
                    const int q = l15 >> 2, p = lane & 3;
                    const int ta = (8 * kq + q) * 128 + (((2 * w + (p >> 1)) ^ ((q & 2) | ((kq & 1) << 2))) * 16) + 8 * (p & 1);
#pragma unroll
                    for (int m = 0; m < 2; ++m)
#pragma unroll
                        for (int k = 0; k < 18; ++k) {
                            int tb = ta + m * 73728 + (k >= 9 ? 36864 : 0);
                            const v4s lo = __builtin_amdgcn_ds_read_tr16_b64_v4i16((LAS v4s*)(L + tb + (k % 9) * 4096));
                            const v4s hi = __builtin_amdgcn_ds_read_tr16_b64_v4i16((LAS v4s*)(L + tb + (k % 9) * 4096 + 512));
                            bf[m][k] = __builtin_bit_cast(v4u, __builtin_shufflevector(lo, hi, 0, 1, 2, 3, 4, 5, 6, 7));
                        }
#pragma unroll
                    for (int k = 0; k < 2; ++k) {
                        const v4s lo = __builtin_amdgcn_ds_read_tr16_b64_v4i16((LAS v4s*)(L + (ta + FD) + k * 4096));
                        const v4s hi = __builtin_amdgcn_ds_read_tr16_b64_v4i16((LAS v4s*)(L + (ta + FD) + k * 4096 + 512));
                        ef[k] = __builtin_bit_cast(v4u, __builtin_shufflevector(lo, hi, 0, 1, 2, 3, 4, 5, 6, 7)); PIN(ef[k]);
                    }
#pragma unroll
                    for (int m = 0; m < 2; ++m)
#pragma unroll
                        for (int k = 0; k < 18; ++k) PIN(bf[m][k]);
                }
                int aa = FA + ((lane & 3) == 3 ? 0 : (lane & 3)) * FROW + kq * 16, ga = FG + (16 * w + l15) * 2, fs = FS;
                PIN(fs);
                int gra = FG + kq * 16, ho = l15 == 0 ? WS_H6 + f * 2304 + 2048 + w * 64 + kq * 16 : OOB;
                PIN(aa); PIN(ga); PIN(gra); PIN(ho);
                STAMP(13);
                PAGE();
                v4u av[18];
                RAWBAR();
                STAMP(7);
                v4f ag = {0.f, 0.f, 0.f, 0.f}, au = {0.f, 0.f, 0.f, 0.f}, s1v, s2v; float mean, rs;
#pragma unroll
                for (int k = 0; k < 6; ++k) av[k] = *(LAS v4u*)(L + aa + k * 64);
                SCHED();
#pragma unroll
                for (int k = 0; k < 18; ++k) {
                    ag = mfma16(av[k], bf[0][k], ag); au = mfma16(av[k], bf[1][k], au);
                    if (k + 6 < 18) av[k + 6] = *(LAS v4u*)(L + aa + (k + 6) * 64);
                    if (k == 8) { RAWBAR(); s1v = *(LAS v4f*)(L + fs); s2v = *(LAS v4f*)(L + fs + 16); }
                    if (k == 11) { mean = sum4(s1v) * (1.f / M); PIN(mean); }
                    if (k == 12) { rs = sum4(s2v) * (1.f / M); PIN(rs); }
                    if (k == 13) { rs = rb(fmaxf(rs - mean * mean, 0.f)); PIN(rs); }
                    if (k == 14) { rs = __builtin_amdgcn_sqrtf(rb(rs + 1e-5f)); PIN(rs); }
                    if (k == 15) { rs = __builtin_amdgcn_rcpf(rb(rs)); PIN(rs); }
                    if (k == 16) { mean = -rb(mean) * rs; PIN(mean); }
                    SCHED();
                }
                const unsigned gu = pkbf(rs * ag.x + (mean * ag.y + ag.z), rs * au.x + (mean * au.y + au.z));
                const float a1 = bflo(gu), a2 = bfhi(gu);
                constexpr float K0 = -2.f * 1.4426950408889634f * 0.7978845608028654f, K1 = K0 * 0.044715f;
                const float e = __builtin_amdgcn_exp2f(a1 * (K0 + K1 * a1 * a1));
                const float ge = rb(a1 * __builtin_amdgcn_rcpf(1.f + e));
                *(LAS u16*)(L + ga) = (u16)pkbf(ge * a2, 0.f);
                BAR();
                STAMP(14);
                const v4u g0 = *(LAS v4u*)(L + gra), g1 = *(LAS v4u*)(L + gra + 64);
                const v4f y0 = {0.f, 0.f, 0.f, 0.f};
                st_sc1(__builtin_bit_cast(v4u, mfma16(ef[1], g1, mfma16(ef[0], g0, y0))), rws, ho);
                STAMP(8);

            } else {
                const int v = w - 4;
                v4u dr[2][8];
                {
                    const rsrc_t rd = RSRC(wd, M * FF * 2);
                    const int dofs = (64 * f + 8 * kq) * 1152 + v * 256 + l15 * 16;
                    RAWBAR();
#pragma unroll
                    for (int u = 0; u < 2; ++u)
#pragma unroll
                        for (int e = 0; e < 8; ++e) dr[u][e] = __builtin_amdgcn_raw_buffer_load_b128(rd, dofs, (32 * u + e) * 1152, AUX_NT);
                    SCHED();
                    RAWBAR();
                }
                const bool on = lane < 36;
                const int cg = 36 * v + lane;
                v4f xv, gav, c1v;
                int to = on ? WS_T5 + cg * 16 : OOB, ua = on ? FA + cg * 8 : FG + 128 + lane * 8;
                {
                    const int mo = on ? WS_MRB + cg * 8 : OOB;
                    const v2u xr = __builtin_amdgcn_raw_buffer_load_b64(rx, on ? cg * 8 : OOB, 0, 0);
                    v2u gr, sr, cr;
#pragma unroll 1
                    for (int sp = 0;; ++sp) {
                        MEMBAR();
                        gr = __builtin_amdgcn_raw_buffer_load_b64(rws, mo, 0, AUX_SC1); sr = __builtin_amdgcn_raw_buffer_load_b64(rws, mo, 1152, AUX_SC1); cr = __builtin_amdgcn_raw_buffer_load_b64(rws, mo, 2304, AUX_SC1);
                        if (__all(umax(umax(gr.x, sr.x), cr.x) != POISON) || sp > SPIN_MAX) break;
                        __builtin_amdgcn_s_sleep(4);
                    }
                    if (on) { *(LAS v2u*)(L + ua + FROW) = cr; *(LAS v2u*)(L + ua + 2 * FROW) = sr; }
                    const unsigned x0 = xr.x, x1 = xr.y, g0 = gr.x, g1 = gr.y, c0 = cr.x, c1 = cr.y;
                    xv = v4f{bflo(x0), bfhi(x0), bflo(x1), bfhi(x1)}; gav = v4f{bflo(g0), bfhi(g0), bflo(g1), bfhi(g1)}; c1v = v4f{bflo(c0), bfhi(c0), bflo(c1), bfhi(c1)};
                    PIN(xv); PIN(gav); PIN(c1v); PIN(to); PIN(ua);
                }
                STAMP(6);
                int sa = FS + half * 16 + v * 4, gra = FG + kq * 16, ho = lane < 32 ? WS_H6 + f * 2304 + v * 512 + l15 * 32 + kq * 16 : OOB;
                PIN(sa); PIN(gra); PIN(ho);
                v4u df[2][8];
#pragma unroll
                for (int u = 0; u < 2; ++u) {
#pragma unroll
                    for (int p = 0; p < 4; ++p) interleave(dr[u][2 * p], dr[u][2 * p + 1]);
#pragma unroll
                    for (int n = 0; n < 8; ++n) { df[u][n] = v4u{dr[u][n >> 2][n & 3], dr[u][2 + (n >> 2)][n & 3], dr[u][4 + (n >> 2)][n & 3], dr[u][6 + (n >> 2)][n & 3]}; PIN(df[u][n]); }
                }
                STAMP(13);
                PAGE();
                cue<false>(rws, WS_CTX + f * 512 + v * 128, 0, CUE7);
                __builtin_amdgcn_s_setprio(2);
                v4f x2;
                int spin = SPIN_MAX;
#pragma unroll 1
                do {
                    MEMBAR();
                    v4u t4[4];
#pragma unroll
                    for (int t = 0; t < 4; ++t) t4[t] = __builtin_amdgcn_raw_buffer_load_b128(rws, to, 2304 * t, AUX_SC1);
                    SCHED();
                    VWAIT(0);
                    x2 = (__builtin_bit_cast(v4f, t4[0]) + __builtin_bit_cast(v4f, t4[1])) + (__builtin_bit_cast(v4f, t4[2]) + __builtin_bit_cast(v4f, t4[3]));
                } while (__any(__builtin_isunordered(x2.x + x2.y, x2.z + x2.w)) && --spin);
                STAMP(7);
                x2 = rb4(xv + rb4(gav * rb4(x2)));
                const v4f uv = x2 * c1v;
                *(LAS v2u*)(L + ua) = v2u{pkbf(uv.x, uv.y), pkbf(uv.z, uv.w)};
                SCHED();
                float s = red32(sum4(x2), sum4(x2 * x2));
                SCHED();
                BAR();
                s = row_sum(red16(s, s));
                *(LAS float*)(L + sa) = s;
                __builtin_amdgcn_s_setprio(0);
                BAR();
                RAWBAR();
                STAMP(14);
                const v4u g0 = *(LAS v4u*)(L + gra), g1 = *(LAS v4u*)(L + gra + 64);
                v4f y[8] = {};
#pragma unroll
                for (int n = 0; n < 8; ++n) y[n] = mfma16(g1, df[1][n], mfma16(g0, df[0][n], y[n]));
                const bool up = kq & 1;
                st_sc1(v4u{f2u(up ? y[4].x : y[0].x), f2u(up ? y[5].x : y[1].x), f2u(up ? y[6].x : y[2].x), f2u(up ? y[7].x : y[3].x)}, rws, ho);
                STAMP(8);
            }
        } else if (b >= 20) {
            const int o6 = b - 20, kg = o6 / 9, nt = o6 - 9 * kg;
            const int head = 8 * kg + w;
            v4u of[4][4];
            {
                const rsrc_t ro = RSRC(wo, 4096 * M * 2);
                const int oo = (128 * head + 8 * kq) * 1152 + nt * 128 + l15 * 8;
                v2u orr[4][8];
#pragma unroll
                for (int u = 0; u < 4; ++u)
#pragma unroll
                    for (int e = 0; e < 8; ++e) orr[u][e] = ld64_nt(ro, oo, (32 * u + e) * 1152);
                SCHED();
#pragma unroll
                for (int u = 0; u < 4; ++u) frag4(orr[u], of[u]);
            }
            STAMP(6);
            STAMP(13);
            int co = WS_CTX + ((head >> 1) * 8 + (lane & 7)) * 512 + (head & 1) * 256 + (lane & 8) * 8 + kq * 16;
            int ra = (l15 * 4 + 2 * half + (kq & 1)) * 48 + w * 4, rr = lane * 48, to = WS_T5 + kg * 2304 + nt * 256 + lane * 4;
            PIN(co); PIN(ra); PIN(rr); PIN(to);
            v4u a0 = ld_sc1(rws, co), a1 = ld_sc1(rws, co + 128);
            SCHED();
            VWAIT(0);
            int spin = SPIN_MAX;
#pragma unroll 1
            while (__any(umax(umax(a0.x, a0.z), umax(a1.x, a1.z)) == POISON) && --spin) {
                MEMBAR();
                if (umax(umax(a0.x, a0.z), umax(a1.x, a1.z)) == POISON) { a0 = ld_sc1(rws, co); a1 = ld_sc1(rws, co + 128); }
            }
            STAMP(7);
            v4f ae[4] = {}, ao[4] = {};
#pragma unroll
            for (int n = 0; n < 4; ++n) { ae[n] = mfma16(a1, of[2][n], mfma16(a0, of[0][n], ae[n])); ao[n] = mfma16(a1, of[3][n], mfma16(a0, of[1][n], ao[n])); }
            float c[4];
#pragma unroll
            for (int n = 0; n < 4; ++n) c[n] = sum4(half ? ao[n] : ae[n]);
            *(LAS float*)(L + ra) = red16(red32(c[0], c[2]), red32(c[1], c[3]));
            BAR();
            if (w == 0) {
                const v4f p0 = *(LAS v4f*)(L + rr), p1 = *(LAS v4f*)(L + rr + 16);
                st32_sc1(f2u(sum4(p0 + p1)), rws, to);
            }
            STAMP(8);
        } else if (b < 18) {
            {
                v4u mw[18]; unsigned mc[18];
#pragma unroll
                for (int i = 0; i < 18; ++i) mc[i] = __builtin_amdgcn_raw_buffer_load_b16(rc, (72 * w + 4 * i + kq) * 2, 0, 0);
#pragma unroll
                for (int i = 0; i < 18; ++i) mw[i] = ld_nt(rmod, (72 * w + 4 * i + kq) * 6912 + 2304 + b * 256 + l15 * 16);
                SCHED();
#pragma unroll
                for (int i = 0; i < 18; ++i) { mc[i] = f2u(silu_bf(bflo(mc[i]))); PIN(mc[i]); }
                ZERO8(acc);
#pragma unroll
                for (int i = 0; i < 18; ++i) dot8z(acc, mw[i], mc[i] >> 16, mc[i]);
                const float b0 = red32(acc[0], acc[4]), b1 = red32(acc[1], acc[5]), b2 = red32(acc[2], acc[6]), b3 = red32(acc[3], acc[7]);
                v2f c; c.x = red16(b0, b2); c.y = red16(b1, b3);
                *(LAS v2f*)(red + w * 128 + l15 * 8 + kq * 2) = c;
                BAR();
                if (tid < 32) {
                    v4f s = *(LAS v4f*)(red + tid * 4);
#pragma unroll
                    for (int k = 1; k < 8; ++k) s += *(LAS v4f*)(red + k * 128 + tid * 4);
                    const int col = 128 * b + 4 * tid;
                    const float one = (col >= 1152 && col < 1728) ? 1.f : 0.f;
                    st64_sc1(v2u{pkbf(rb(s.x) + one, rb(s.y) + one), pkbf(rb(s.z) + one, rb(s.w) + one)}, rws, WS_MRB + col * 2);
                }
                STAMP(6);
            }
            if (w == 0) {
                const int cg = 8 * b + (lane & 7);
                const int m = 4 * cg + 2 * half + (kq & 1);
                v4u t; unsigned gb, gaw;
                const unsigned xr = __builtin_amdgcn_raw_buffer_load_b16(rx, m * 2, 0, 0);
                cue<false>(rws, WS_CTX + (72 + b) * 512, 0, CUE7);
#pragma unroll 1
                for (int sp = 0;; ++sp) {
                    MEMBAR();
                    gb = ld32_sc1(rws, WS_MRB + 3 * 1152 + (m & ~1) * 2); gaw = ld32_sc1(rws, WS_MRB + (m & ~1) * 2);
                    t = ld_sc1(rws, WS_T5 + kq * 2304 + cg * 16);
                    if (__all(umax(umax(umax(t.x, t.y), umax(t.z, t.w)), umax(gb, gaw)) != POISON) || sp > SPIN_MAX) break;
                    __builtin_amdgcn_s_sleep(1);
                }
                const unsigned t0 = t.x, t1 = t.y, t2 = t.z, t3 = t.w;
                float x2 = rb(bflo(xr) + rb(((kq & 1) ? bfhi(gaw) : bflo(gaw)) * rb(red16(red32(u2f(t0), u2f(t2)), red32(u2f(t1), u2f(t3))))));
                float gsel = (kq & 1) ? bfhi(gb) : bflo(gb);
                PIN(x2); PIN(gsel);
                STAMP(7);
                { const u64 t0 = rt(); while (rt() - t0 < (u64)CUE8) __builtin_amdgcn_s_sleep(1); }
                v4f s;
                int spin = SPIN_MAX;
#pragma unroll 1
                do {
                    MEMBAR();
                    v4u y[9];
#pragma unroll
                    for (int i = 0; i < 9; ++i) y[i] = ld_sc1(rws, WS_H6 + (8 * i + (lane >> 3)) * 2304 + cg * 16);
                    SCHED();
                    VWAIT(FINW);
                    s = __builtin_bit_cast(v4f, y[0]);
#pragma unroll
                    for (int i = 1; i < 9; ++i) s += __builtin_bit_cast(v4f, y[i]);
                } while (__any(s.x != s.x) && --spin);
                const float yv = rb(add_x8(red16(red32(s.x, s.z), red32(s.y, s.w))));
                const float o = x2 + rb(gsel * yv);
                if ((lane & 8) == 0) out[m] = (u16)pkbf(o, o);
                if (b == 0 && lane == 0) st32_sc1(tag, rws, WS_EPOCH);
                STAMP(8);
            }
        }
    } else {
        unsigned f1, tag;
        int f1o = WS_CL + (lane & 3) * 256; PIN(f1o);
        kvo = (lane ^ (((w & 3) << 2) | ((w >> 2) & 3))) * 16; kvs = (a * 8 + w) * 1024; kvl = w * 1024;
#pragma unroll 1
        for (int n = pn; n < UKA; n += 128) {
            asm volatile("s_waitcnt vmcnt(%0)" :: "i"(PFW - 1) : "memory");
            dma16<0>(RSRC(KPTR(ka0, 3), DC * 8192), ASCR + w * 1024, po, UK_ROW(n) * 8192);
        }
        STAMP(1);
        KV_LOADS(6, DMAW);
        asm volatile("global_load_dword %0, %2, %4 sc1\n\tglobal_load_dword %1, %3, %4 sc1" : "=&v"(f1), "=&v"(tag) : "v"(f1o), "v"(WS_EPOCH), "s"(ws) : "memory");
        const int hpair = a >> 3, cq = a & 7;
        unsigned ad[8];
        if (w < 4) {
            const int r = lane & 31, g = ((r & 3) << 2) | ((r >> 2) & 3);
#pragma unroll
            for (int j = 0; j < 8; ++j) { ad[j] = w * 32768 + r * 1024 + (((2 * j + half) ^ g) * 16); PIN(ad[j]); }
        } else {
            const int blk = kq & 1, q = l15 >> 2, p = lane & 3;
#pragma unroll
            for (int j = 0; j < 8; ++j) {
                const int t = j >> 1, tt = j & 1, g = (q << 2) | ((2 * tt + half) & 3);
                ad[j] = (8 * tt + 4 * half + q) * 1024 + (w - 4) * 256 + (((4 * t + 2 * blk + (p >> 1)) ^ g) * 16) + 8 * (p & 1); PIN(ad[j]);
            }
        }
        int qv4[4], qs4[4];
#pragma unroll
        for (int i = 0; i < 4; ++i) { const int ir = (i + (a >> 3)) & 3; qv4[i] = (lane ^ ((ir << 2) | (w & 3))) * 16; PIN(qv4[i]); qs4[i] = (4 * w + ir) * 1024; }
        int qc[4];
#pragma unroll
        for (int i = 0; i < 4; ++i) { qc[i] = QOFF + lane * 16 + qs4[i]; PIN(qc[i]); }
        static_assert(DMAW == 4, "the waits below count on it");
        KV_LOADS(4, DMAW);
        asm volatile("" : "+v"(f1), "+v"(tag));
        tag += 1u;
        if (__builtin_expect(!__all(f1 == tag), 0)) {
#pragma unroll 1
            for (int sp = 0; sp < SPIN_MAX; ++sp) { MEMBAR(); if (__all(ld32_sc1(rws, f1o) == tag)) break; }
        }
#define Q_LOADS() do { _Pragma("unroll") for (int i = 0; i < 4; ++i) dma16<2>(rws, QOFF + qs4[i], qv4[i], WS_QA + qs4[i]); } while (0)
#define Q_TO_LDS() do { \
            KV_LOADS(3, DMAW); \
            const bool e2 = rt() - t_start >= (u64)TQ; \
            if (e2) Q_LOADS(); \
            KV_LOADS(3, DMAW); \
            STAMP(2); \
            const bool e3 = !e2 && rt() - t_start >= (u64)TQ2; \
            if (e3) { Q_LOADS(); asm volatile("s_waitcnt vmcnt(4)" ::: "memory"); } else DRAIN(); \
            RAWBAR();     \
            STAMP(11); \
            if (!e2 && !e3) { while (rt() - t_start < (u64)TQ2) {} Q_LOADS(); } \
            PREREAD(); \
            int spin = SPIN_MAX; \
            _Pragma("unroll 1") for (;;) { \
                DRAIN(); \
                unsigned pz[4]; \
                _Pragma("unroll") for (int i = 0; i < 4; ++i) { const v4u q = *(LAS v4u*)(L + qc[i]); pz[i] = umax(q.x, q.z); } \
                if (__builtin_expect(__all(umax(umax(pz[0], pz[1]), umax(pz[2], pz[3])) != POISON), 1) || !--spin) break; \
                _Pragma("unroll") for (int i = 0; i < 4; ++i) if (pz[i] == POISON) dma16<2>(rws, QOFF + qs4[i], qv4[i], WS_QA + qs4[i]); \
            } \
            STAMP(12); \
        } while (0)
        constexpr int OST = QOFF + 16384, OFL = QOFF + 15360;
        if (w < 4) {
            PAGE();
            v4u kf[32], qf[32];
#define PREREAD() do { _Pragma("unroll") for (int s = 0; s < KPRE; ++s) kf[s] = *(LAS v4u*)(L + ad[s & 7] + (s >> 3) * 256); SCHED(); } while (0)
            int mxr = QOFF + 8192 + (lane & 31) * 16, mxw = mxr + w * 4, mtr = QOFF + 9216 + (lane & 31) * 48, mtw = mtr + w * 8, pwa = QOFF + 2 * w * 1024 + lane * 16;
            PIN(mxr); PIN(mxw); PIN(mtr); PIN(mtw); PIN(pwa);
            unsigned aq[8];
#pragma unroll
            for (int j = 0; j < 8; ++j) { aq[j] = ad[j] + (QOFF - w * 32768); PIN(aq[j]); }
            int r0 = OST + w * 2048 + lane * 16, r1 = kq * 1024 + w * 256 + l15 * 16, so = WS_INB + ((lane >> 3) * 8 + 2 * w) * 32768 + a * 256 + (lane & 7) * 16;
            int co = (lane & 6) ? OOB : WS_CK + (a * 8 + 2 * w) * 128 + ((lane >> 3) + 8 * (lane & 1)) * 8, fl = OFL + w * 4;
            int spo = (lane & 33) ? OOB : WS_ST + a * 256 + lane * 8, sto = WS_ST + (32 * w + (lane & 31)) * 256 + hpair * 16;
            PIN(r0); PIN(r1); PIN(so); PIN(co); PIN(fl); PIN(spo); PIN(sto);
            Q_TO_LDS();
            BAR();
            STAMP(3);
            v16f acc = {};
            {
                constexpr int PD = 6;
#pragma unroll
                for (int s = 0; s < PD; ++s) qf[s] = *(LAS v4u*)(L + aq[s & 7] + (s >> 3) * 256);
                SCHED();
#pragma unroll
                for (int s = 0; s < 32; ++s) {
                    acc = mfma32(kf[s], qf[s], acc);
                    if (s + PD < 32) qf[s + PD] = *(LAS v4u*)(L + aq[(s + PD) & 7] + ((s + PD) >> 3) * 256);
                    constexpr int NK = 32 - KPRE;
                    if (s < 24 && (s + 1) * NK / 24 > s * NK / 24) { const int k2 = KPRE + s * NK / 24; kf[k2] = *(LAS v4u*)(L + ad[k2 & 7] + (k2 >> 3) * 256); }
                    SCHED();
                }
            }
            RAWBAR();
            SCHED();
            STAMP(13);
            float m = -3.0e38f;
#pragma unroll
            for (int i = 0; i < 16; ++i) m = fmaxf(m, acc[i]);
            {
                auto sw = __builtin_amdgcn_permlane32_swap(f2u(m), f2u(m), false, false); const unsigned x0 = sw[0], x1 = sw[1];
                m = __builtin_ceilf(fmaxf(fmaxf(-3.0e38f, u2f(x0)), u2f(x1)));
            }
            *(LAS v2u*)(L + mtw) = v2u{f2u(m), tag};
            SCHED();
            float p[16];
#pragma unroll
            for (int i = 0; i < 16; ++i) p[i] = acc[i] - m;
            SCHED();
#pragma unroll
            for (int i = 0; i < 8; ++i) p[i] = __builtin_amdgcn_exp2f(p[i]);
            SCHED();
            MEMBAR();
            v4u mk0 = *(LAS v4u*)(L + mtr), mk1 = *(LAS v4u*)(L + mtr + 16);
            SCHED();
#pragma unroll
            for (int i = 8; i < 16; ++i) p[i] = __builtin_amdgcn_exp2f(p[i]);
            unsigned pw[8];
#pragma unroll
            for (int i = 0; i < 8; ++i) pw[i] = pkbf(p[2 * i], p[2 * i + 1]);
            PIN8(pw, 0);
            SCHED();
#define MK_BAD() __any((((mk0.y ^ tag) | (mk0.w ^ tag)) | ((mk1.y ^ tag) | (mk1.w ^ tag))) != 0u)
            if (__builtin_expect(MK_BAD(), 0)) {
#pragma unroll 1
                do { MEMBAR(); mk0 = *(volatile LAS v4u*)(L + mtr); mk1 = *(volatile LAS v4u*)(L + mtr + 16); } while (MK_BAD());
            }
            const unsigned k0 = mk0.x, k1 = mk0.z, k2 = mk1.x, k3 = mk1.z;
            const float mm = fmaxf(fmaxf(fmaxf(fmaxf(m, u2f(k0)), u2f(k1)), u2f(k2)), u2f(k3));
            const float dm = fminf(mm - m, 255.f);
            const unsigned sub = (unsigned)(dm * 8388736.f);
#pragma unroll
            for (int h2 = 0; h2 < 2; ++h2) {
#pragma unroll
                for (int i = 4 * h2; i < 4 * h2 + 4; ++i) pw[i] = __builtin_bit_cast(unsigned, __builtin_elementwise_sub_sat(__builtin_bit_cast(v2us, pw[i]), __builtin_bit_cast(v2us, sub)));
                *(LAS v4u*)(L + pwa + 1024 * h2) = v4u{pw[4 * h2], pw[4 * h2 + 1], pw[4 * h2 + 2], pw[4 * h2 + 3]};
                SCHED();
            }
            SCHED();
            BAR();
            SCHED();
            STAMP(4);
            float l = 0.f;
#pragma unroll
            for (int i = 0; i < 16; ++i) l += p[i];
            l = red32(l, l) * __builtin_amdgcn_exp2f(-dm);
            *(LAS float*)(L + mxw + 512) = l;
            BAR();
            const unsigned tb2 = (unsigned)rt();
            if (w == 1) {
                const float ls = u2f(((f2u(sum4(*(LAS v4f*)(L + mxr + 512))) + 0x800u) & 0xFFFFF000u) | (((lane & 1) ? tb2 << 2 : tb2 << 6) & 0xF00u));
                st_sc1(pack4(mm, dpp<0xB1>(mm), ls, dpp<0xB1>(ls), tag), rws, spo);
            }
            {
                unsigned cs[2][2];
#pragma unroll
                for (int t = 0; t < 4; ++t) {
                    v4u o0, o1;
                    if (t < 3) {
                        unsigned fv;
                        asm volatile("1:\n\tds_read_b32 %2, %3 offset:%6\n\tds_read_b128 %0, %4 offset:%7\n\tds_read_b128 %1, %4 offset:%8\n\ts_waitcnt lgkmcnt(0)\n\tv_cmp_ne_u32 vcc, %2, %5\n\ts_cbranch_vccnz 1b"
                                     : "=&v"(o0), "=&v"(o1), "=&v"(fv) : "v"(fl), "v"(t ? r1 : r0), "v"(tag), "i"(t * 16), "i"(t * 8192), "i"(t ? t * 8192 + 4096 : 1024) : "vcc", "memory");
                    } else {
                        RAWBAR();
                        o0 = *(LAS v4u*)(L + r1 + 3 * 8192); o1 = *(LAS v4u*)(L + r1 + 3 * 8192 + 4096);
                    }
                    __builtin_amdgcn_raw_buffer_store_b128(o0, rws, so + (t >> 1) * 128, (t & 1) * 32768, AUX_SC1);
                    __builtin_amdgcn_raw_buffer_store_b128(o1, rws, so + (t >> 1) * 128, (64 + (t & 1)) * 32768, AUX_SC1);
                    SCHED(); asm volatile("s_nop 1"); SCHED();
                    unsigned c0 = (o0.x + o0.y) + (o0.z + o0.w), c1 = (o1.x + o1.y) + (o1.z + o1.w);
                    if (t >> 1) {
                        c0 = addu_dpp<0xB1>(c0 + cs[t & 1][0]); c1 = addu_dpp<0xB1>(c1 + cs[t & 1][1]);
                        unsigned c = addu_dpp<0x4E>((lane & 1) ? c1 : c0);
                        c += (unsigned)__builtin_amdgcn_update_dpp(0, __builtin_amdgcn_update_dpp(0, (int)c, 0x1B, 0xf, 0xf, true), 0x141, 0xf, 0xf, true);
                        st64_sc1(v2u{c, tag}, rws, co + (t & 1) * 128);
                    } else { PIN(c0); PIN(c1); cs[t][0] = c0; cs[t][1] = c1; }
                    SCHED();
                }
                STAMP(5);
            }
            const u64 tship = rt();
            v4u s0 = ld_sc1(rws, sto);
            SCHED();
            PAGE();
            int io = WS_INB + a * 32768 + (32 * w + 8 * kq) * 256 + l15 * 8;
            int wtw = QOFF + 12288 + w * 128 + lane * 2, wtr = QOFF + 12288 + w * 128 + (lane & 1) * 64 + kq * 16, mw = QOFF + 12800 + half * 16;
            int cko = lane < 32 ? WS_CK + ((32 * w + lane) * 8 + cq) * 128 + hpair * 8 : OOB;
            int law = QOFF + 13312 + (kq & 1) * 512 + w * 128 + half * 64 + l15 * 4;
            PIN(io); PIN(wtw); PIN(wtr); PIN(mw); PIN(cko); PIN(law);
            v2u d[2][8];
#define D_LOADS(p) _Pragma("unroll") for (int e = 0; e < 8; ++e) { d[p][e] = __builtin_amdgcn_raw_buffer_load_b64(rws, io + e * 256 + (p) * 128, 0, AUX_SC1); SCHED(); }
            if (tb2 - (unsigned)t_start < LATE_B2) { while (rt() - tship < (u64)CUE4) {} }
            D_LOADS(0);
            PIN(s0);
            if (__builtin_expect(!__all(s0.w == tag), 0)) {
#pragma unroll 1
                for (int sp = 0; sp < SPIN_MAX; ++sp) { MEMBAR(); s0 = ld_sc1(rws, sto); if (__all(s0.w == tag)) break; }
            }
            {
                const int dt = (int)(signed char)((((s0.z >> 8) & 15u) | ((s0.x & 15u) << 4)) - (tb2 >> 2));
                if (__builtin_expect(__any(dt > LAG_OK), 0)) {
                    const unsigned due = tb2 + 4 * dt + LAG_DUE;
#pragma unroll 1
                    for (int sp = 0; sp < 4096 && __any((int)(due - (unsigned)rt()) > 0); ++sp) {}
                    const unsigned lm = (unsigned)__builtin_amdgcn_ballot_w64(dt > LAG_OK) >> (8 * kq);
#pragma unroll
                    for (int e = 0; e < 8; ++e) if ((lm >> e) & 1u) d[0][e] = __builtin_amdgcn_raw_buffer_load_b64(rws, io + e * 256, 0, AUX_SC1);
                }
            }
            STAMP(6);
            D_LOADS(1);
            v2u ck = __builtin_amdgcn_raw_buffer_load_b64(rws, cko, 0, AUX_SC1);
            SCHED();
            v4u wa;
            {
                const v4f sv = unpack4(s0);
                const float ms = half ? sv.y : sv.x, lv = u2f(f2u(half ? sv.w : sv.z) & 0xFFFFF000u);
                *(LAS float*)(L + mw + w * 4) = row_max(max16(ms));
                BAR();
                const v4f mk2 = *(LAS v4f*)(L + mw);
                const float e = rb(__builtin_amdgcn_exp2f(ms - fmaxf(fmaxf(fmaxf(fmaxf(ms, mk2.x), mk2.y), mk2.z), mk2.w)));
                *(LAS u16*)(L + wtw) = (u16)(f2u(e) >> 16);
                float ll = e * lv; ll = row_sum(red16(ll, ll));
                *(LAS float*)(L + mw + 32 + w * 4) = ll;
                LGKM0();
                wa = *(LAS v4u*)(L + wtr);
                PIN(wa);
            }
            STAMP(14);
            unsigned lo1;
#define COMBINE(ok) do { \
                unsigned sm = 0, pk[2][2]; const v4f z = {0.f, 0.f, 0.f, 0.f}; \
                _Pragma("unroll") for (int p = 0; p < 2; ++p) { \
                    v4u lo[2], hi[2]; \
                    _Pragma("unroll") for (int p2 = 0; p2 < 4; ++p2) { \
                        _Pragma("unroll") for (int t = 0; t < 2; ++t) { const unsigned u0 = d[p][2 * p2][t], u1 = d[p][2 * p2 + 1][t]; lo[t][p2] = perm(u1, u0, 0x05040100u); hi[t][p2] = perm(u1, u0, 0x07060302u); sm += u0 + u1; } \
                        SCHED(); } \
                    pk[p][0] = pkbf(mfma16(wa, lo[0], z).x, mfma16(wa, lo[1], z).x); pk[p][1] = pkbf(mfma16(wa, hi[0], z).y, mfma16(wa, hi[1], z).y); \
                    if (p == 0) { pk[0][0] = (kq & 1) ? pk[0][1] : pk[0][0]; PIN(pk[0][0]); SCHED(); } } \
                sm -= ck.x; \
                sm = addu_dpp<0x124>(addu_dpp<0x128>(addu_dpp<0x4E>(addu_dpp<0xB1>(sm)))); \
                sm = redu16(sm, sm); sm = redu32(sm, sm); \
                ok = __all(sm == 0u && (ck.y == tag || lane >= 32)); \
                lo1 = half ? ((kq & 1) ? pk[1][1] : pk[1][0]) : pk[0][0]; \
            } while (0)
            bool ok;
            COMBINE(ok);
            if (__builtin_expect(!ok, 0)) {
#pragma unroll 1
                for (int sp = 0; sp < SPIN_MAX && !ok; ++sp) {
#pragma unroll 1
                    for (int s2 = 0; s2 < SPIN_MAX && !__all(ck.y == tag || lane >= 32); ++s2) { MEMBAR(); ck = __builtin_amdgcn_raw_buffer_load_b64(rws, cko, 0, AUX_SC1); }
                    MEMBAR();
                    D_LOADS(0); D_LOADS(1);
                    COMBINE(ok);
                }
            }
            *(LAS unsigned*)(L + law) = lo1;
            STAMP(7);
            BAR();
        } else {
            PAGE();
            const int v = w - 4;
            const int col = lane & 31;
            int ob0 = OST + v * 2048 + 2 * half * 128 + col * 4, ob1 = half * 1024 + v * 256 + col * 4, fl = OFL + v * 4, pra = QOFF + lane * 16;
            const rsrc_t rv = RSRC(wuv, DC * 8192);
            int vo = (128 * (cq >> 1) + 32 * (cq & 1) + 8 * kq) * 8192 + hpair * 512 + v * 128 + l15 * 8;
            unsigned ah[8];
#pragma unroll
            for (int j = 0; j < 8; ++j) { ah[j] = ad[j] + 65536; PIN(ah[j]); }
            int ob0y = ob0 + 128, ob1y = ob1 + 128;
            PIN(ob0); PIN(ob1); PIN(ob0y); PIN(ob1y); PIN(fl); PIN(pra); PIN(vo);
#undef PREREAD
#define PREREAD()
            Q_TO_LDS();
            BAR();
            v2u vr[2][8];
#pragma unroll
            for (int i = 0; i < UVA; ++i) vr[i >> 3][i & 7] = ld64_nt(rv, vo, (64 * (i >> 3) + (i & 7)) * 8192);
            SCHED();
            v4s vb[4][16];
#define VRD(t, k) vb[t][k] = __builtin_amdgcn_ds_read_tr16_b64_v4i16((LAS v4s*)(L + (((k) >> 3) ? ah[2 * (t) + ((k) & 1)] : ad[2 * (t) + ((k) & 1)]) + (((k) >> 1) & 3) * 16384))
#define VREAD(t) _Pragma("unroll") for (int k = 0; k < 16; ++k) VRD(t, k)
            VREAD(0);
            SCHED();
            RAWBAR();
#pragma unroll
            for (int i = UVA; i < 16; ++i) vr[i >> 3][i & 7] = ld64_nt(rv, vo, (64 * (i >> 3) + (i & 7)) * 8192);
            SCHED();
            VREAD(1);
            __builtin_amdgcn_s_setprio(2);
            BAR();
            STAMP(4);
            v4u pa[8];
#pragma unroll
            for (int ks = 0; ks < 8; ++ks) pa[ks] = *(LAS v4u*)(L + pra + ks * 1024);
            SCHED();
            v16f acc[4];
#define OSTORE2(t, g) do { unsigned u0_, u1_; \
                asm volatile("v_cvt_pk_bf16_f32 %0, %2, %3\n\tv_cvt_pk_bf16_f32 %1, %4, %5\n\tds_write2st64_b32 %6, %0, %1 offset0:%7 offset1:%8" : "=&v"(u0_), "=&v"(u1_) \
                    : "v"(acc[t][8 * ((g) >> 1) + 2 * ((g) & 1)]), "v"(acc[t][8 * ((g) >> 1) + 2 * ((g) & 1) + 1]), "v"(acc[t][8 * ((g) >> 1) + 2 * ((g) & 1) + 4]), "v"(acc[t][8 * ((g) >> 1) + 2 * ((g) & 1) + 5]), \
                      "v"((t) ? (((g) & 1) ? ob1y : ob1) : (((g) & 1) ? ob0y : ob0)), "i"((t) ? (t) * 32 + ((g) >> 1) * 16 : ((g) >> 1) * 4), "i"((t) ? (t) * 32 + ((g) >> 1) * 16 + 8 : ((g) >> 1) * 4 + 2) : "memory"); } while (0)
#pragma unroll
            for (int t = 0; t < 4; ++t) {
#pragma unroll
                for (int ks = 0; ks < 8; ++ks) {
                    const v4u bv = __builtin_bit_cast(v4u, __builtin_shufflevector(vb[t][2 * ks], vb[t][2 * ks + 1], 0, 1, 2, 3, 4, 5, 6, 7));
                    const v16f z = {};
                    acc[t] = mfma32(pa[ks], bv, ks ? acc[t] : z);
                    if (t < 2) { VRD(t + 2, 2 * ks); VRD(t + 2, 2 * ks + 1); }
                    if (t > 0 && ks >= 2 && ks < 6) { SCHED(); OSTORE2(t - 1, ks - 2); }
                    if (t > 0 && ks == 5) { MEMBAR(); *(volatile LAS unsigned*)(L + fl + (t - 1) * 16) = tag; }
                    if (t == 0 && ks == 5) RAWBAR();
                    SCHED();
                }
            }
            STAMP(13);
            asm volatile("s_nop 11" : "+v"(acc[3]));
#pragma unroll
            for (int g = 0; g < 4; ++g) OSTORE2(3, g);
            BAR();
            __builtin_amdgcn_s_setprio(0);
            RAWBAR();
            v4u vf[2][4];
            frag4(vr[0], vf[0]); frag4(vr[1], vf[1]);
            int lr = QOFF + 13312 + (v >> 1) * 512 + (lane & 3) * 128 + kq * 16, cxo = lane < 16 ? WS_CTX + a * 512 + v * 128 + l15 * 8 : OOB, lo = QOFF + 12832 + (v >> 1) * 16;
            PIN(lr); PIN(cxo); PIN(lo);
            RAWBAR();
            {
                const v4u a0 = *(LAS v4u*)(L + lr), a1 = *(LAS v4u*)(L + lr + 64);
                const v4f l4 = *(LAS v4f*)(L + lo);
                SCHED();
                const float rl = __builtin_amdgcn_rcpf(sum4(l4));
                v4f y[4] = {};
#pragma unroll
                for (int n = 0; n < 4; ++n) y[n] = mfma16(a1, vf[1][n], mfma16(a0, vf[0][n], y[n]));
                st64_sc1(v2u{pkbf(sum4(y[0]) * rl, sum4(y[1]) * rl), pkbf(sum4(y[2]) * rl, sum4(y[3]) * rl)}, rws, cxo);
            }
            STAMP(8);
        }
    }
    STAMP_FLUSH();
}

extern "C" void kernel_launch(void* const* d_in, const int* in_sizes, int n_in, void* d_out, int out_size, void* d_ws, size_t ws_size, hipStream_t stream) {
    fused_kernel<<<256, NT, 0, stream>>>((const u16*)d_in[0], (const u16*)d_in[1], (const u16*)d_in[2], (const u16*)d_in[3], (const u16*)d_in[4], (const u16*)d_in[5],
                                         (const u16*)d_in[7], (const u16*)d_in[8], (const u16*)d_in[9], (const u16*)d_in[10], (const u16*)d_in[11], (u16*)d_out, (unsigned char*)d_ws + WS_BASE);
}
```
